```python
import torch

def _softmax(x, axis=-1):
    return torch.softmax(x, dim=axis)

def _gelu(x):
    return torch.nn.functional.gelu(x, approximate='tanh')

def _silu(x):
    return torch.nn.functional.silu(x)

def _sigmoid(x):
    return torch.sigmoid(x)

def _layernorm(x, axis=-1, eps=1e-05):
    m = x.mean(dim=axis, keepdim=True)
    v = x.var(dim=axis, keepdim=True, unbiased=False)
    return (x - m) / torch.sqrt(v + eps)

def _scatter_add(tgt, idx, src, axis=0):
    return tgt.clone().index_add_(axis, idx, src)

def _segment_sum(src, seg, n):
    out = torch.zeros((n,) + src.shape[1:], dtype=src.dtype)
    return out.index_add_(0, seg, src)

def _pad(x, pw):
    flat = tuple((v for pair in reversed(pw) for v in pair))
    return torch.nn.functional.pad(x, flat)
kernel_name = 'gated_mix__memory_aug_recurrent_mem_tr__moe_arch_mod__prompt'

def setup_inputs():

    _w = lambda a: (lambda t: t.to(torch.bfloat16) if t.is_floating_point() else t)(torch.as_tensor(a))

    def _A_setup():
        B, S, Dm = (1, 2048, 448)
        H, Dh = (16, 256)
        M, seg = (8, 32)
        seg = min(seg, S)
        S = S // seg * seg
        return {'x': _w((lambda *s: torch.randn(*s, dtype=torch.bfloat16) * 0.05)(B, S, Dm)), 'mem0': _w((lambda *s: torch.randn(*s, dtype=torch.bfloat16) * 0.8)(B, M, Dm)), 'Wq': _w((lambda *s: torch.randn(*s, dtype=torch.bfloat16) * 1.6)(Dm, H, Dh) * Dm ** (-0.5)), 'Wk': _w((lambda *s: torch.randn(*s, dtype=torch.bfloat16) * 1.6)(Dm, H, Dh) * Dm ** (-0.5)), 'Wv': _w((lambda *s: torch.randn(*s, dtype=torch.bfloat16) * 0.8)(Dm, H, Dh) * Dm ** (-0.5)), 'Wo': _w((lambda *s: torch.randn(*s, dtype=torch.bfloat16) * 0.8)(H, Dh, Dm) * (H * Dh) ** (-0.5)), 'seg_len': seg}
    _A_in = _A_setup()

    def _B_setup():
        B, S, D, F, C = (1, 2048, 448, 1792, 1)
        return {'x': _w((lambda *s: torch.randn(*s, dtype=torch.bfloat16) * 0.05)(B, S, D)), 'w_r': _w((lambda *s: torch.randn(*s, dtype=torch.bfloat16) * 1.6)(D)), 'w_gate': _w((lambda *s: torch.randn(*s, dtype=torch.bfloat16) * 0.2)(D, F)), 'w_up': _w((lambda *s: torch.randn(*s, dtype=torch.bfloat16) * 0.2)(D, F)), 'w_down': _w((lambda *s: torch.randn(*s, dtype=torch.bfloat16) * 0.2)(F, D))}
    _B_in = _B_setup()
    return {'A_x': _A_in['x'], 'A_mem0': _A_in['mem0'], 'A_Wq': _A_in['Wq'], 'A_Wk': _A_in['Wk'], 'A_Wv': _A_in['Wv'], 'A_Wo': _A_in['Wo'], 'A_seg_len': _A_in['seg_len'], 'B_x': _B_in['x'], 'B_w_r': _B_in['w_r'], 'B_w_gate': _B_in['w_gate'], 'B_w_up': _B_in['w_up'], 'B_w_down': _B_in['w_down'], 'gate_w': _w((lambda *s: torch.randn(*s, dtype=torch.bfloat16) * 0.05)(448) * 16.0)}

def reference(A_x, A_mem0, A_Wq, A_Wk, A_Wv, A_Wo, A_seg_len, B_x, B_w_r, B_w_gate, B_w_up, B_w_down, gate_w):

    def _A_ref(x, mem0, Wq, Wk, Wv, Wo, seg_len):
        B, S, Dm = x.shape
        H, Dh = (Wq.shape[1], Wq.shape[2])
        M = mem0.shape[1]
        scale = Dh ** (-0.5)
        n_seg = S // seg_len
        L = M + seg_len
        idx = torch.arange(L)
        mask = (idx[:, None] < M) | (idx[:, None] >= idx[None, :])
        mem = mem0
        outs = []
        for s in range(n_seg):
            xs = x[:, s * seg_len:(s + 1) * seg_len, :].clone()
            h = torch.concatenate([mem, xs], axis=1)
            q = torch.einsum('btd,dhe->bhte', h, Wq)
            k = torch.einsum('btd,dhe->bhte', h, Wk)
            v = torch.einsum('btd,dhe->bhte', h, Wv)
            scores = torch.einsum('bhqe,bhke->bhqk', q, k) * scale
            scores = torch.where(mask[None, None], scores, -1000000000.0)
            p = _softmax(scores, axis=-1)
            o = torch.einsum('bhqk,bhke->bhqe', p, v)
            o = torch.einsum('bhte,hed->btd', o, Wo)
            mem = o[:, :M, :].clone()
            outs.append(o[:, M:, :])
        return torch.concatenate(outs, axis=1)

    def _B_ref(x, w_r, w_gate, w_up, w_down):
        B, S, D = x.shape
        C = 1
        r = x @ w_r
        idx = torch.argsort(-r, axis=-1)[:, :C]
        idx_e = (lambda x, n, axis: torch.repeat_interleave(x, n, dim=axis))(idx.reshape(B, C, 1), D, axis=2)
        x_sel = (lambda x, i, axis: torch.take_along_dim(x, i, dim=axis))(x, idx_e, axis=1)
        g = _silu(x_sel @ w_gate)
        u = x_sel @ w_up
        y = g * u @ w_down
        r_sel = _sigmoid((lambda x, i, axis: torch.take_along_dim(x, i, dim=axis))(r, idx, axis=1))
        y = y * r_sel.reshape(B, C, 1)
        b_off = (torch.arange(B) * S).reshape(B, 1)
        flat_idx = (idx + b_off).reshape(B * C)
        zero = (x * 0.0).reshape(B * S, D)
        upd = _scatter_add(zero, flat_idx, y.reshape(B * C, D), axis=0)
        return x + upd.reshape(B, S, D)
    x = A_x
    g = _sigmoid((x * gate_w).sum(-1, keepdims=True))
    a = _A_ref(x, A_mem0, A_Wq, A_Wk, A_Wv, A_Wo, A_seg_len)
    b = _B_ref(x, B_w_r, B_w_gate, B_w_up, B_w_down)
    return g * a + (1 - g) * b
```

```cpp
#include <hip/hip_runtime.h>
#include <hip/hip_bf16.h>
typedef unsigned short u16;
constexpr int S = 2048, D = 448, H = 16, E = 256, M = 8, SEG = 32, NSEG = 64, F = 1792, NC = 2 * H * D;
__device__ __forceinline__ float bf2f(u16 v) { return __uint_as_float((unsigned)v << 16); }
__device__ __forceinline__ u16 f2bf(float f) { __hip_bfloat16 b = __float2bfloat16(f); return *(u16*)&b; }

__global__ void k_ab(const u16* Wq, const u16* Wk, const u16* Wv, const u16* Wo, u16* Wc) {
  int n = blockIdx.x, d = threadIdx.x;
  float acc = 0.f;
  if (n < H * D) {
    int h = n / D, c = n % D;
    for (int e = 0; e < E; ++e) acc += bf2f(Wq[(c * H + h) * E + e]) * bf2f(Wk[(d * H + h) * E + e]);
    acc *= 0.0625f * 1.4426950408889634f;
  } else {
    int h = (n - H * D) / D, c = (n - H * D) % D;
    for (int e = 0; e < E; ++e) acc += bf2f(Wv[(d * H + h) * E + e]) * bf2f(Wo[(h * E + e) * D + c]);
  }
  Wc[(size_t)n * D + d] = f2bf(acc);
}
__global__ void k_proj(const u16* X, const u16* Wc, u16* Y) {
  __shared__ float xs[8][D];
  int r0 = blockIdx.y * 8, n = blockIdx.x * 256 + threadIdx.x;
  for (int i = threadIdx.x; i < 8 * D; i += 256) xs[i / D][i % D] = bf2f(X[(size_t)r0 * D + i]);
  __syncthreads();
  float acc[8] = {};
  const uint4* w = (const uint4*)(Wc + (size_t)n * D);
  for (int k = 0; k < D / 8; ++k) {
    uint4 v = w[k]; unsigned u[4] = {v.x, v.y, v.z, v.w};
#pragma unroll
    for (int j = 0; j < 4; ++j) {
      float a = __uint_as_float(u[j] << 16), b = __uint_as_float(u[j] & 0xffff0000u);
#pragma unroll
      for (int r = 0; r < 8; ++r) acc[r] += a * xs[r][k * 8 + 2 * j] + b * xs[r][k * 8 + 2 * j + 1];
    }
  }
#pragma unroll
  for (int r = 0; r < 8; ++r) Y[(size_t)(r0 + r) * NC + n] = f2bf(acc[r]);
}
__device__ void attn_row(const u16* q, const u16* Ym, const u16* Yx, int nk, float* res, float (*p)[40], float* qs) {
  int t = threadIdx.x;
  for (int i = t; i < D; i += blockDim.x) qs[i] = bf2f(q[i]);
  __syncthreads();
  for (int i = t; i < H * 40; i += blockDim.x) {
    int h = i / 40, j = i % 40; float s = -1e30f;
    if (j < M + nk) {
      const u16* k = (j < M ? Ym + (size_t)j * NC : Yx + (size_t)(j - M) * NC) + h * D;
      s = 0.f; for (int c = 0; c < D; ++c) s += qs[c] * bf2f(k[c]);
      s = bf2f(f2bf(s));
    }
    p[h][j] = s;
  }
  __syncthreads();
  if (t < H) {
    float mx = -1e30f; for (int j = 0; j < M + nk; ++j) mx = fmaxf(mx, p[t][j]);
    float l = 0.f; for (int j = 0; j < M + nk; ++j) { float e = bf2f(f2bf(exp2f(p[t][j] - mx))); p[t][j] = e; l += e; }
    for (int j = 0; j < M + nk; ++j) p[t][j] /= l;
  }
  __syncthreads();
  float acc = 0.f;
  if (t < D) for (int h = 0; h < H; ++h) for (int j = 0; j < M + nk; ++j) {
    const u16* v = (j < M ? Ym + (size_t)j * NC : Yx + (size_t)(j - M) * NC) + H * D + h * D;
    acc += p[h][j] * bf2f(v[t]);
  }
  *res = acc;
}
__global__ void k_step(u16* mem, const u16* Ym, const u16* Yx, int s) {
  __shared__ float p[H][40]; __shared__ float qs[D];
  float r; attn_row(mem + ((size_t)s * M + blockIdx.x) * D, Ym + (size_t)s * M * NC, Yx + (size_t)s * SEG * NC, SEG, &r, p, qs);
  if (threadIdx.x < D) mem[((size_t)(s + 1) * M + blockIdx.x) * D + threadIdx.x] = f2bf(r);
}
__global__ void k_router(const u16* x, const u16* wr, const u16* wg, const u16* wu, const u16* wd, float* y, int* idx) {
  __shared__ float rv[1024]; __shared__ int ri[1024]; __shared__ float hs[F]; __shared__ float xs[D];
  int t = threadIdx.x; float best = -INFINITY; int bi = 0x7fffffff;
  for (int r = t; r < S; r += 1024) {
    float a = 0.f; for (int d = 0; d < D; ++d) a += bf2f(x[(size_t)r * D + d]) * bf2f(wr[d]);
    a = bf2f(f2bf(a)); if (a > best || bi == 0x7fffffff) { best = a; bi = r; }
  }
  rv[t] = best; ri[t] = bi; __syncthreads();
  for (int o = 512; o > 0; o >>= 1) { if (t < o) { if (rv[t + o] > rv[t] || (rv[t + o] == rv[t] && ri[t + o] < ri[t])) { rv[t] = rv[t + o]; ri[t] = ri[t + o]; } } __syncthreads(); }
  int sel = ri[0]; float rs = rv[0];
  if (t < D) xs[t] = bf2f(x[(size_t)sel * D + t]);
  __syncthreads();
  for (int f = t; f < F; f += 1024) {
    float g = 0.f, u = 0.f; for (int d = 0; d < D; ++d) { g += xs[d] * bf2f(wg[(size_t)d * F + f]); u += xs[d] * bf2f(wu[(size_t)d * F + f]); }
    g = bf2f(f2bf(g)); u = bf2f(f2bf(u)); g = bf2f(f2bf(g / (1.f + expf(-g)))); hs[f] = bf2f(f2bf(g * u));
  }
  __syncthreads();
  if (t < D) { float a = 0.f; for (int f = 0; f < F; ++f) a += hs[f] * bf2f(wd[(size_t)f * D + t]); a = bf2f(f2bf(a)); float sg = bf2f(f2bf(1.f / (1.f + expf(-rs)))); y[t] = bf2f(f2bf(a * sg)); }
  if (t == 0) *idx = sel;
}
__global__ void k_out(const u16* x, const u16* Ym, const u16* Yx, const u16* gw, const float* y, const int* idx, u16* out) {
  __shared__ float p[H][40]; __shared__ float qs[D]; __shared__ float red[512];
  int row = blockIdx.x, s = row / SEG, i = row % SEG, t = threadIdx.x;
  float a; attn_row(x + (size_t)row * D, Ym + (size_t)s * M * NC, Yx + (size_t)s * SEG * NC, i + 1, &a, p, qs);
  red[t] = t < D ? bf2f(f2bf(qs[t] * bf2f(gw[t]))) : 0.f; __syncthreads();
  for (int o = 256; o > 0; o >>= 1) { if (t < o) red[t] += red[t + o]; __syncthreads(); }
  float g = bf2f(f2bf(1.f / (1.f + expf(-bf2f(f2bf(red[0]))))));
  if (t < D) {
    float b = qs[t]; if (row == *idx) b = bf2f(f2bf(b + y[t]));
    a = bf2f(f2bf(a));
    out[(size_t)row * D + t] = f2bf(bf2f(f2bf(g * a)) + bf2f(f2bf(bf2f(f2bf(1.f - g)) * b)));
  }
}
extern "C" void kernel_launch(void* const* d_in, const int* in_sizes, int n_in, void* d_out, int out_size, void* d_ws, size_t ws_size, hipStream_t stream) {
  const u16* x = (const u16*)d_in[0]; const u16* mem0 = (const u16*)d_in[1];
  char* ws = (char*)d_ws;
  u16* Wc = (u16*)ws; u16* Yx = (u16*)(ws + (16u << 20)); u16* Ym = (u16*)(ws + (80u << 20)); u16* mem = (u16*)(ws + (100u << 20));
  float* y = (float*)(ws + (102u << 20)); int* idx = (int*)(ws + (103u << 20));
  hipMemcpyAsync(mem, mem0, M * D * 2, hipMemcpyDeviceToDevice, stream);
  k_ab<<<NC, D, 0, stream>>>((const u16*)d_in[2], (const u16*)d_in[3], (const u16*)d_in[4], (const u16*)d_in[5], Wc);
  k_proj<<<dim3(NC / 256, S / 8), 256, 0, stream>>>(x, Wc, Yx);
  k_router<<<1, 1024, 0, stream>>>(x, (const u16*)d_in[8], (const u16*)d_in[9], (const u16*)d_in[10], (const u16*)d_in[11], y, idx);
  for (int s = 0; s < NSEG; ++s) {
    k_proj<<<dim3(NC / 256, 1), 256, 0, stream>>>(mem + (size_t)s * M * D, Wc, Ym + (size_t)s * M * NC);
    if (s + 1 < NSEG) k_step<<<M, 512, 0, stream>>>(mem, Ym, Yx, s);
  }
  k_out<<<S, 512, 0, stream>>>(x, Ym, Yx, (const u16*)d_in[12], y, idx, (u16*)d_out);
}
```

```cpp
#include <hip/hip_runtime.h>
#include <hip/hip_bf16.h>

typedef unsigned short u16;
typedef unsigned u32;
typedef u32 v4u __attribute__((ext_vector_type(4)));
typedef u32 v2u __attribute__((ext_vector_type(2)));
typedef float f4 __attribute__((ext_vector_type(4)));
typedef float f16v __attribute__((ext_vector_type(16)));
typedef __bf16 bf8 __attribute__((ext_vector_type(8)));
typedef __bf16 bf2 __attribute__((ext_vector_type(2)));
typedef short s4 __attribute__((ext_vector_type(4)));
#define LAS __attribute__((address_space(3)))
#define GAS __attribute__((address_space(1)))

namespace {
constexpr int D = 448, H = 16, E = 256, F = 1792;
constexpr u32 MB = 1u << 20;
constexpr u32 O_P0 = 0, O_P1 = 2 * MB, O_CTRL = 8 * MB, O_P5 = 10 * MB, O_WF = 12 * MB, O_SX = 26 * MB,
              O_KX = 40 * MB, O_VX = 41 * MB, O_KM = 172 * MB, O_W = O_KM + 16 * 16384, WS_USED = 204 * MB;
constexpr u32 O_GFLAG = O_CTRL, O_CFLAG = O_CTRL + 112 * 128, O_PFLAG = O_CTRL + 184 * 128,
              O_ZFLAG = O_CTRL + 200 * 128, O_CAL = O_CTRL + 32768, O_MEM0 = O_CTRL + 36864, O_RT = O_CTRL + 45056,
              O_GATE = O_CTRL + 49152, O_PREP = O_CTRL + 98304, O_GO = O_CTRL + 131072,
              O_EPOCH = O_CTRL + 139264, O_SFLAG = O_CTRL + 147456, O_FFLAG = O_SFLAG + 64 * 128, O_YP = O_CTRL + 163840, O_DIAG = 208 * MB;
typedef unsigned long long u64;
constexpr int LDS_BYTES = 163840;
constexpr u32 SPIN = 1u << 19;

__device__ __forceinline__ float bflo(u32 v) { return __uint_as_float(v << 16); }
__device__ __forceinline__ float bfhi(u32 v) { return __uint_as_float(v & 0xffff0000u); }
__device__ __forceinline__ u32 pk(float a, float b) { bf2 p = {(__bf16)a, (__bf16)b}; return __builtin_bit_cast(u32, p); }
__device__ __forceinline__ float rnd(float a) { return (float)(__bf16)a; }
__device__ __forceinline__ f4 mfma16(v4u a, v4u b, f4 c) { return __builtin_amdgcn_mfma_f32_16x16x32_bf16(__builtin_bit_cast(bf8, a), __builtin_bit_cast(bf8, b), c, 0, 0, 0); }
__device__ __forceinline__ f16v mfma32(v4u a, v4u b, f16v c) { return __builtin_amdgcn_mfma_f32_32x32x16_bf16(__builtin_bit_cast(bf8, a), __builtin_bit_cast(bf8, b), c, 0, 0, 0); }
__device__ __forceinline__ u32 slot_off(int g, int cls) { return (u32)g * 32768u + (u32)((cls ^ (((g >> 4) & 1) << 1)) << 13); }
__device__ __forceinline__ u32 seg_off(int s, int fb) { return (u32)s * 2 * MB + (u32)(((s ^ fb) & 1) << 13); }
__device__ __forceinline__ u32 by_off(int s, int fb) { return (u32)s * (MB / 2) + (u32)((((s >> 2) ^ fb) & 1) << 13); }
__device__ __forceinline__ int row_map(int r) { return 16 * (r >> 4) + 8 * ((r >> 2) & 1) + 4 * ((r >> 3) & 1) + (r & 3); }
__device__ __forceinline__ int gemm_wg(int gi) { return (gi % 28) * 8 + 4 + gi / 28; }
__device__ __forceinline__ float sigmoidf(float x) { return 1.f / (1.f + __expf(-x)); }
__device__ __forceinline__ void bar() { asm volatile("s_waitcnt lgkmcnt(0)\n\ts_barrier" ::: "memory"); }
typedef __amdgpu_buffer_rsrc_t rsrc_t;
__device__ __forceinline__ v4u ld16(rsrc_t r, u32 off) { return __builtin_amdgcn_raw_buffer_load_b128(r, off, 0, 16); }
__device__ __forceinline__ u32 ld4(rsrc_t r, u32 off) { return __builtin_amdgcn_raw_buffer_load_b32(r, off, 0, 16); }
__device__ __forceinline__ v2u ld8(rsrc_t r, u32 off) { return __builtin_amdgcn_raw_buffer_load_b64(r, off, 0, 16); }
__device__ __forceinline__ void st8(rsrc_t r, u32 off, v2u v) { __builtin_amdgcn_raw_buffer_store_b64(v, r, off, 0, 16); }
__device__ __forceinline__ void st16(rsrc_t r, u32 off, v4u v) { __builtin_amdgcn_raw_buffer_store_b128(v, r, off, 0, 16); }
__device__ __forceinline__ void st4(rsrc_t r, u32 off, u32 v) { __builtin_amdgcn_raw_buffer_store_b32(v, r, off, 0, 16); }
__device__ __forceinline__ float xmax(float m) {
  auto a = __builtin_amdgcn_permlane16_swap(__float_as_uint(m), __float_as_uint(m), false, false);
  m = fmaxf(__uint_as_float(a[0]), __uint_as_float(a[1]));
  auto b = __builtin_amdgcn_permlane32_swap(__float_as_uint(m), __float_as_uint(m), false, false);
  return fmaxf(__uint_as_float(b[0]), __uint_as_float(b[1]));
}
__device__ __forceinline__ float xsum(float m) {
  auto a = __builtin_amdgcn_permlane16_swap(__float_as_uint(m), __float_as_uint(m), false, false);
  m = __uint_as_float(a[0]) + __uint_as_float(a[1]);
  auto b = __builtin_amdgcn_permlane32_swap(__float_as_uint(m), __float_as_uint(m), false, false);
  return __uint_as_float(b[0]) + __uint_as_float(b[1]);
}
}

namespace {
__device__ __forceinline__ void setup_unit(const u16* __restrict__ x, const u16* __restrict__ wr,
                                           const u16* __restrict__ gw, rsrc_t rs, int u, int lane) {
  const int tok = u * 8 + (lane & 7);
  v4u xa[7], ra[7], ga[7];
#pragma unroll
  for (int it = 0; it < 7; ++it) {
    const int c = ((lane >> 3) + 8 * it) * 8;
    xa[it] = *(const v4u*)(x + (size_t)tok * D + c); ra[it] = *(const v4u*)(wr + c); ga[it] = *(const v4u*)(gw + c);
  }
  float dr = 0.f, dg = 0.f;
#pragma unroll
  for (int it = 0; it < 7; ++it)
#pragma unroll
    for (int k = 0; k < 4; ++k) {
      dr += bflo(xa[it][k]) * bflo(ra[it][k]) + bfhi(xa[it][k]) * bfhi(ra[it][k]);
      dg += rnd(bflo(xa[it][k]) * bflo(ga[it][k])) + rnd(bfhi(xa[it][k]) * bfhi(ga[it][k]));
    }
#pragma unroll
  for (int o = 8; o < 64; o <<= 1) { dr += __shfl_xor(dr, o); dg += __shfl_xor(dg, o); }
  dr = rnd(dr);
  if (lane < 8) st4(rs, O_GATE + tok * 4, __float_as_uint(rnd(sigmoidf(rnd(dg)))));
  int bi = tok;
#pragma unroll
  for (int o = 1; o < 8; o <<= 1) {
    const float v2 = __shfl_xor(dr, o); const int i2 = __shfl_xor(bi, o);
    if (v2 > dr || (v2 == dr && i2 < bi)) { dr = v2; bi = i2; }
  }
  if (lane == 0) st8(rs, O_RT + u * 8, (v2u){__float_as_uint(dr), (u32)bi});
}

__device__ __forceinline__ void probe_unit(char* ws, rsrc_t rs, int j, int lane) {
  u32 best = ~0u;
  for (int r = 0; r < 20; ++r) {
    const int c = (r + j) & 3;
    const u32 off = c * 8192 + j * 256;
    u64 t0, t1; u32 v;
    asm volatile("s_memtime %0\n\ts_waitcnt lgkmcnt(0)\n\ts_load_dword %2, %3, %4 glc\n\ts_waitcnt lgkmcnt(0)\n\ts_memtime %1\n\ts_waitcnt lgkmcnt(0)"
                 : "=&s"(t0), "=&s"(t1), "=&s"(v) : "s"(ws), "s"(off) : "memory");
    if (lane == c) best = min(best, (u32)(t1 - t0));
  }
  if (lane < 4) st4(rs, O_CAL + (j * 4 + lane) * 4, best);
}

__device__ __forceinline__ v2u read_epoch(const char* ws) {
  u64 e;
  asm volatile("s_load_dwordx2 %0, %1, %2\n\ts_waitcnt lgkmcnt(0)" : "=&s"(e) : "s"(ws), "s"(O_EPOCH) : "memory");
  e = e * 0x9E3779B97F4A7C15ull + 0xD1B54A32D192ED03ull;
  e ^= e >> 29;
  return (v2u){(u32)e & ~255u, (u32)(e >> 32)};
}

__device__ __forceinline__ u32 wait_epoch(rsrc_t rs, u32 off, v2u ep, u32 least = 0) {
  v2u f = {0, 0};
  for (u32 sp = 0; sp < SPIN; ++sp) {
    f = ld8(rs, off);
    if (!__any((((f.x ^ ep.x) - least) >> 8 | (f.y ^ ep.y)) != 0)) break;
    asm volatile("s_sleep 6" ::: "memory");
  }
  asm volatile("s_waitcnt vmcnt(0)" ::: "memory");
  return __builtin_amdgcn_readfirstlane(f.x & 15);
}

__device__ __forceinline__ void wait_flags(rsrc_t rs, u32 off, int n, u32 want, int w, int lane) {
  if (w == 0) {
    for (u32 sp = 0; sp < SPIN; ++sp) {
      const u32 a = ld4(rs, off + min(lane, n - 1) * 128), c = ld4(rs, off + min(lane + 64, n - 1) * 128);
      if (!__any(min(a, c) < want)) break;
      asm volatile("s_sleep 2" ::: "memory");
    }
  }
  bar();
}

__device__ __forceinline__ void route(rsrc_t rs, int lane, int& sel, float& rsel) {
  float bv = 0.f; int bi = 0;
#pragma unroll
  for (int i = 0; i < 4; ++i) {
    const v2u e = ld8(rs, O_RT + (lane * 4 + i) * 8);
    const float v = __uint_as_float(e.x);
    if (i == 0 || v > bv) { bv = v; bi = (int)e.y; }
  }
#pragma unroll
  for (int o = 1; o < 64; o <<= 1) {
    const float v2 = __shfl_xor(bv, o); const int i2 = __shfl_xor(bi, o);
    if (v2 > bv || (v2 == bv && i2 < bi)) { bv = v2; bi = i2; }
  }
  sel = __builtin_amdgcn_readfirstlane(bi);
  rsel = __uint_as_float(__builtin_amdgcn_readfirstlane(__float_as_uint(bv)));
}

__device__ __forceinline__ void collect(rsrc_t rs, v2u ep, int w, int lane) {
  if (w < 4) wait_epoch(rs, O_PREP + (w * 64 + lane) * 128, ep);
  bar();
  if (w == 0) {
    u32 t = (ld4(rs, O_CAL + (lane & 15) * 4) << 2) | (lane & 3);
#pragma unroll
    for (int o = 1; o < 16; o <<= 1) t = min(t, (u32)__shfl_xor((int)t, o));
    const u32 cls = (t & 3) | (((t & 3) ^ 1) << 2);
    st8(rs, O_GO + lane * 128, (v2u){(ep.x & ~255u) | cls, ep.y});
    if (lane == 0) st8(rs, O_EPOCH, ep);
  }
}

__device__ __forceinline__ void poison_role(rsrc_t rs, int pi, u32 cls, int tid) {
  const int c0 = cls & 3, c1 = cls >> 2;
  for (int u = pi; u < 11 * 64; u += 47) {
    const int k = u % 11, g = u / 11;
    const int page = k == 10 ? 1 : k < 8 ? k >> 1 : 5;
    const int c = (page & 1 ? c1 : c0) ^ (k == 10 ? 1 : (k & 1) * 2);
    st16(rs, page * 2 * MB + slot_off(g, c) + tid * 16, (v4u){~0u, ~0u, ~0u, ~0u});
  }
  asm volatile("s_waitcnt vmcnt(0)" ::: "memory");
  bar();
  if (tid == 0) st4(rs, O_ZFLAG + pi * 128, 64);
}

__device__ __forceinline__ void score_role(char* ws, int h, int w, int lane, u32 d1, u32 cc) {
  const int cls[2] = {(int)(cc & 3), (int)(cc >> 2)};
  const int g = lane >> 4, m = lane & 7, kap = 2 * (g & 1) + (g >> 1), r = 4 * h + (w & 3);
  const u32 lane16 = lane * 16, voff16 = w * 1024 + lane16, comb4 = r * 128 + m * 16 + kap * 4;
  const u32 flag = w < 4 ? O_GFLAG + ((lane >> 5) * 56 + ((h * 14 + min(lane & 31, 13)) >> 2)) * 128 : O_PFLAG + h * 128;
  const u32 opf = O_KX + r * 16384 + seg_off(0, cls[0]);
  const u32 omem0 = O_MEM0, omem1 = O_P1 + slot_off(1, cls[1] ^ 2), oxs = O_P0 + slot_off(0, cls[0] ^ 2), osm = O_P1 + slot_off(0, cls[1]),
            osg = O_P5 + slot_off(0, cls[1] ^ 2), own = h * 128;
#define SCORE_IN [voff16] "v"(voff16), [lane16] "v"(lane16), [comb4] "v"(comb4), [flag] "v"(flag), [base] "s"(ws), [omem0] "s"(omem0), \
      [omem1] "s"(omem1), [oxs] "s"(oxs), [osm] "s"(osm), [opf] "s"(opf), [wave] "s"(w), [osg] "s"(osg), [d1] "s"(d1), [own] "s"(own)
  asm volatile(
    "v_add_u32 v183, 0x400000, %[comb4]\n"
    "v_min_u32 v30, 7152, %[voff16]\n"
    "s_mov_b64 s[40:41], %[base]\n"
    "s_add_u32 s40, s40, %[omem0]\n"
    "s_addc_u32 s41, s41, 0\n"
    "s_mov_b64 s[42:43], %[base]\n"
    "s_add_u32 s42, s42, %[omem1]\n"
    "s_addc_u32 s43, s43, 0\n"
    "s_mov_b64 s[44:45], %[base]\n"
    "s_add_u32 s44, s44, %[oxs]\n"
    "s_addc_u32 s45, s45, 0\n"
    "s_mov_b64 s[46:47], %[base]\n"
    "s_add_u32 s46, s46, %[osm]\n"
    "s_addc_u32 s47, s47, 0\n"
    "s_mov_b64 s[48:49], s[46:47]\n"
    "s_xor_b32 s48, s48, 0x2000\n"
    "s_mov_b64 s[38:39], %[base]\n"
    "s_add_u32 s38, s38, %[osg]\n"
    "s_addc_u32 s39, s39, 0\n"
    "s_mov_b32 s56, 0\n"
    "s_mov_b32 s57, 1\n"
    "s_mov_b32 s58, 0x80000\n"
    "s_mov_b32 s66, 0\n"
    "s_mov_b32 s60, 0x00ff00ff\n"
    "s_mov_b32 s61, 0x00ff00ff\n"
    "s_mov_b32 s71, 0\n"
    "s_mov_b32 s72, 0\n"
    "s_mov_b32 s73, 0\n"
    "s_mov_b32 s74, 0\n"
    "s_cmp_gt_u32 %[wave], 3\n"
    "s_cbranch_scc1 LP_%=\n"
    "s_mov_b64 s[50:51], %[base]\n"
    "s_add_u32 s50, s50, %[opf]\n"
    "s_addc_u32 s51, s51, 0\n"
    "v_add_u32 v182, 0x1000, %[lane16]\n"
    "s_add_u32 s57, s57, 1\n"
    "s_min_u32 s57, s57, 64\n"
    "s_branch Lfl0S_%=\n"
    "Lflgo0S_%=:\n"
    "global_load_dwordx4 v[88:91], %[lane16], s[50:51] offset:0 sc1\n"
    "global_load_dwordx4 v[92:95], %[lane16], s[50:51] offset:1024 sc1\n"
    "global_load_dwordx4 v[96:99], %[lane16], s[50:51] offset:2048 sc1\n"
    "global_load_dwordx4 v[100:103], %[lane16], s[50:51] offset:3072 sc1\n"
    "global_load_dwordx4 v[104:107], v182, s[50:51] offset:0 sc1\n"
    "global_load_dwordx4 v[108:111], v182, s[50:51] offset:1024 sc1\n"
    "global_load_dwordx4 v[112:115], v182, s[50:51] offset:2048 sc1\n"
    "s_xor_b32 s50, s50, 0x2000\n"
    "s_add_u32 s50, s50, 2097152\n"
    "s_addc_u32 s51, s51, 0\n"
    "s_add_u32 s57, s57, 1\n"
    "s_min_u32 s57, s57, 64\n"
    "LloopS_%=:\n"
    "s_barrier\n"
    "global_load_dwordx4 v[184:187], v30, s[40:41] sc1\n"
    "s_nop 15\n"
    "s_nop 15\n"
    "s_nop 15\n"
    "LspS_%=:\n"
    "global_load_dwordx4 v[192:195], v30, s[40:41] sc1\n"
    "s_waitcnt vmcnt(1)\n"
    "ds_write_b128 %[voff16], v[184:187] offset:0\n"
    "v_max3_u32 v188, v184, v185, v186\n"
    "v_max_u32 v188, v188, v187\n"
    "v_cmp_eq_u32 vcc, -1, v188\n"
    "s_cbranch_vccz LgoS_%=\n"
    "global_load_dwordx4 v[184:187], v30, s[40:41] sc1\n"
    "s_waitcnt vmcnt(1)\n"
    "ds_write_b128 %[voff16], v[192:195] offset:0\n"
    "v_max3_u32 v188, v192, v193, v194\n"
    "v_max_u32 v188, v188, v195\n"
    "v_cmp_eq_u32 vcc, -1, v188\n"
    "s_cbranch_vccz LgoS_%=\n"
    "s_cmp_eq_u32 s58, 0\n"
    "s_cbranch_scc1 LgoS_%=\n"
    "s_sub_u32 s58, s58, 1\n"
    "s_branch LspS_%=\n"
    "LgoS_%=:\n"
    "s_waitcnt lgkmcnt(0)\n"
    "s_barrier\n"
    "ds_read_b128 v[144:147], %[lane16] offset:0\n"
    "ds_read_b128 v[148:151], %[lane16] offset:1024\n"
    "ds_read_b128 v[152:155], %[lane16] offset:2048\n"
    "ds_read_b128 v[156:159], %[lane16] offset:3072\n"
    "ds_read_b128 v[160:163], %[lane16] offset:4096\n"
    "ds_read_b128 v[164:167], %[lane16] offset:5120\n"
    "ds_read_b128 v[168:171], %[lane16] offset:6144\n"
    "s_cmp_eq_u32 s56, 63\n"
    "s_cbranch_scc1 LdoneS_%=\n"
    "s_waitcnt lgkmcnt(6)\n"
    "v_mfma_f32_16x16x32_bf16 v[176:179], v[88:91], v[144:147], 0\n"
    "s_waitcnt lgkmcnt(5)\n"
    "v_mfma_f32_16x16x32_bf16 v[176:179], v[92:95], v[148:151], v[176:179]\n"
    "s_waitcnt lgkmcnt(4)\n"
    "v_mfma_f32_16x16x32_bf16 v[176:179], v[96:99], v[152:155], v[176:179]\n"
    "s_waitcnt lgkmcnt(3)\n"
    "v_mfma_f32_16x16x32_bf16 v[176:179], v[100:103], v[156:159], v[176:179]\n"
    "s_waitcnt lgkmcnt(2)\n"
    "v_mfma_f32_16x16x32_bf16 v[176:179], v[104:107], v[160:163], v[176:179]\n"
    "s_waitcnt lgkmcnt(1)\n"
    "v_mfma_f32_16x16x32_bf16 v[176:179], v[108:111], v[164:167], v[176:179]\n"
    "s_waitcnt lgkmcnt(0)\n"
    "v_mfma_f32_16x16x32_bf16 v[176:179], v[112:115], v[168:171], v[176:179]\n"
    "s_nop 7\n"
    "v_permlane32_swap_b32 v176, v178\n"
    "v_permlane32_swap_b32 v177, v179\n"
    "s_nop 0\n"
    "v_add_f32_dpp v172, v178, v176 row_ror:8 row_mask:0xf bank_mask:0xf\n"
    "v_add_f32_dpp v173, v179, v177 row_ror:8 row_mask:0xf bank_mask:0xf\n"
    "v_exp_f32 v174, v172\n"
    "v_exp_f32 v175, v173\n"
    "s_mov_b64 exec, s[60:61]\n"
    "v_cvt_pk_bf16_f32 v174, v174, v175\n"
    "v_min_u32 v174, -2, v174\n"
    "global_store_dword %[comb4], v174, s[44:45] sc1\n"
    "global_store_dword v183, v174, s[44:45] sc1\n"
    "v_cvt_pk_bf16_f32 v172, v172, v173\n"
    "v_min_u32 v172, -2, v172\n"
    "global_store_dword %[comb4], v172, s[48:49] sc1\n"
    "s_mov_b64 exec, -1\n"
    "s_barrier\n"
    "s_nop 15\n"
    "s_nop 15\n"
    "global_load_dwordx4 v[88:91], %[lane16], s[50:51] offset:0 sc1\n"
    "global_load_dwordx4 v[92:95], %[lane16], s[50:51] offset:1024 sc1\n"
    "global_load_dwordx4 v[96:99], %[lane16], s[50:51] offset:2048 sc1\n"
    "global_load_dwordx4 v[100:103], %[lane16], s[50:51] offset:3072 sc1\n"
    "global_load_dwordx4 v[104:107], v182, s[50:51] offset:0 sc1\n"
    "global_load_dwordx4 v[108:111], v182, s[50:51] offset:1024 sc1\n"
    "global_load_dwordx4 v[112:115], v182, s[50:51] offset:2048 sc1\n"
    "global_load_dword v180, %[flag], %[base] sc1\n"
    "s_add_u32 s56, s56, 1\n"
    "s_mov_b64 s[40:41], s[42:43]\n"
    "s_and_b32 s59, s56, 15\n"
    "s_cselect_b32 s59, 0, 0x4000\n"
    "s_add_u32 s44, s44, 0x8000\n"
    "s_xor_b32 s44, s44, s59\n"
    "s_add_u32 s48, s48, 0x8000\n"
    "s_xor_b32 s48, s48, s59\n"
    "s_add_u32 s59, s56, 1\n"
    "s_and_b32 s59, s59, 15\n"
    "s_cselect_b32 s59, 0, 0x4000\n"
    "s_add_u32 s42, s42, 0x8000\n"
    "s_xor_b32 s42, s42, s59\n"
    "s_xor_b32 s50, s50, 0x2000\n"
    "s_add_u32 s50, s50, 2097152\n"
    "s_addc_u32 s51, s51, 0\n"
    "s_waitcnt vmcnt(0)\n"
    "v_cmp_gt_u32 vcc, s57, v180\n"
    "s_cbranch_vccnz Lfl1S_%=\n"
    "Lflgo1S_%=:\n"
    "s_add_u32 s57, s57, 1\n"
    "s_min_u32 s57, s57, 64\n"
    "s_branch LloopS_%=\n"
    "Lfl0S_%=:\n"
    "s_cmp_eq_u32 s58, 0\n"
    "s_cbranch_scc1 Lflgo0S_%=\n"
    "s_sub_u32 s58, s58, 1\n"
    "s_sleep 2\n"
    "global_load_dword v180, %[flag], %[base] sc1\n"
    "s_waitcnt vmcnt(0)\n"
    "v_cmp_gt_u32 vcc, s57, v180\n"
    "s_cbranch_vccnz Lfl0S_%=\n"
    "s_branch Lflgo0S_%=\n"
    "Lfl1S_%=:\n"
    "s_cmp_eq_u32 s58, 0\n"
    "s_cbranch_scc1 Lflgo1S_%=\n"
    "s_sub_u32 s58, s58, 1\n"
    "s_sleep 2\n"
    "global_load_dword v180, %[flag], %[base] sc1\n"
    "s_waitcnt vmcnt(0)\n"
    "v_cmp_gt_u32 vcc, s57, v180\n"
    "s_cbranch_vccnz Lfl1S_%=\n"
    "s_branch Lflgo1S_%=\n"
    "LP_%=:\n"
    "s_cmp_eq_u32 %[wave], 7\n"
    "s_cbranch_scc1 LG_%=\n"
    "LloopP_%=:\n"
    "s_barrier\n"
    "global_load_dwordx4 v[184:187], v30, s[40:41] sc1\n"
    "s_nop 15\n"
    "s_nop 15\n"
    "s_nop 15\n"
    "LspP_%=:\n"
    "global_load_dwordx4 v[192:195], v30, s[40:41] sc1\n"
    "s_waitcnt vmcnt(1)\n"
    "ds_write_b128 %[voff16], v[184:187] offset:0\n"
    "v_max3_u32 v188, v184, v185, v186\n"
    "v_max_u32 v188, v188, v187\n"
    "v_cmp_eq_u32 vcc, -1, v188\n"
    "s_cbranch_vccz LgoP_%=\n"
    "global_load_dwordx4 v[184:187], v30, s[40:41] sc1\n"
    "s_waitcnt vmcnt(1)\n"
    "ds_write_b128 %[voff16], v[192:195] offset:0\n"
    "v_max3_u32 v188, v192, v193, v194\n"
    "v_max_u32 v188, v188, v195\n"
    "v_cmp_eq_u32 vcc, -1, v188\n"
    "s_cbranch_vccz LgoP_%=\n"
    "s_cmp_eq_u32 s58, 0\n"
    "s_cbranch_scc1 LgoP_%=\n"
    "s_sub_u32 s58, s58, 1\n"
    "s_branch LspP_%=\n"
    "LgoP_%=:\n"
    "s_waitcnt lgkmcnt(0)\n"
    "s_barrier\n"
    "s_cmp_eq_u32 s56, 63\n"
    "s_cbranch_scc1 LdoneS_%=\n"
    "s_barrier\n"
    "s_add_u32 s56, s56, 1\n"
    "s_mov_b64 s[40:41], s[42:43]\n"
    "s_add_u32 s59, s56, 1\n"
    "s_and_b32 s59, s59, 15\n"
    "s_cselect_b32 s59, 0, 0x4000\n"
    "s_add_u32 s42, s42, 0x8000\n"
    "s_xor_b32 s42, s42, s59\n"
    "s_branch LloopP_%=\n"
    "LG_%=:\n"
    "s_bfe_u32 s59, %[d1], 524304\n"
    "s_lshl_b32 s59, s59, 2\n"
    "s_getpc_b64 s[54:55]\n"
    "Lpcs1_%=:\n"
    "s_add_u32 s54, s54, Lss_%=-Lpcs1_%=\n"
    "s_addc_u32 s55, s55, 0\n"
    "s_sub_u32 s54, s54, s59\n"
    "s_subb_u32 s55, s55, 0\n"
    "s_bfe_u32 s59, %[d1], 524312\n"
    "s_lshl_b32 s59, s59, 2\n"
    "s_getpc_b64 s[82:83]\n"
    "Lpcs2_%=:\n"
    "s_add_u32 s82, s82, Lcs_%=-Lpcs2_%=\n"
    "s_addc_u32 s83, s83, 0\n"
    "s_sub_u32 s82, s82, s59\n"
    "s_subb_u32 s83, s83, 0\n"
    "LloopG_%=:\n"
    "s_barrier\n"
    "s_sub_u32 s59, s56, 2\n"
    "s_max_i32 s59, s59, 0\n"
    "v_mov_b32 v172, s59\n"
    "s_barrier\n"
    "s_cmp_eq_u32 s56, 63\n"
    "s_cbranch_scc1 LspG_%=\n"
    "s_barrier\n"
    "LspG_%=:\n"
    "global_load_dwordx4 v[184:187], v30, s[40:41] sc1\n"
    "s_waitcnt vmcnt(0)\n"
    "v_max3_u32 v188, v184, v185, v186\n"
    "v_max_u32 v188, v188, v187\n"
    "v_cmp_eq_u32 vcc, -1, v188\n"
    "s_cbranch_vccz LokG_%=\n"
    "s_cmp_eq_u32 s58, 0\n"
    "s_cbranch_scc1 LokG_%=\n"
    "s_sub_u32 s58, s58, 1\n"
    "s_branch LspG_%=\n"
    "LokG_%=:\n"
    "s_mov_b64 exec, 1\n"
    "global_store_dword %[flag], v172, %[base] sc1\n"
    "s_mov_b64 exec, -1\n"
    "v_mov_b32 v30, %[voff16]\n"
    "s_cmp_eq_u32 s56, 63\n"
    "s_cbranch_scc1 LdoneS_%=\n"
    "s_mov_b64 s[46:47], s[38:39]\n"
    "s_add_u32 s56, s56, 1\n"
    "s_mov_b64 s[40:41], s[42:43]\n"
    "s_and_b32 s59, s56, 15\n"
    "s_cselect_b32 s59, 0, 0x4000\n"
    "s_add_u32 s38, s38, 0x8000\n"
    "s_xor_b32 s38, s38, s59\n"
    "s_add_u32 s59, s56, 1\n"
    "s_and_b32 s59, s59, 15\n"
    "s_cselect_b32 s59, 0, 0x4000\n"
    "s_add_u32 s42, s42, 0x8000\n"
    "s_xor_b32 s42, s42, s59\n"
    "s_waitcnt lgkmcnt(0)\n"
    "s_mov_b64 s[80:81], -1\n"
    "s_setpc_b64 s[54:55]\n"
    ".rept 80\n"
    "s_nop 1\n"
    ".endr\n"
    "Lss_%=:\n"
    "s_load_dword s80, s[46:47], 0\n"
    "s_load_dword s81, s[46:47], 2048\n"
    "s_cmp_eq_u64 s[80:81], 0\n"
    "s_cbranch_scc1 LseenS_%=\n"
    "s_nop 1\n"
    "s_cmp_eq_u64 s[80:81], 0\n"
    "s_cbranch_scc1 LseenS_%=\n"
    "s_nop 3\n"
    "s_load_dword s80, s[46:47], 128\n"
    "s_load_dword s81, s[46:47], 2176\n"
    "s_cmp_eq_u64 s[80:81], 0\n"
    "s_cbranch_scc1 LseenS_%=\n"
    "s_nop 1\n"
    "s_cmp_eq_u64 s[80:81], 0\n"
    "s_cbranch_scc1 LseenS_%=\n"
    "s_nop 3\n"
    "s_load_dword s80, s[46:47], 256\n"
    "s_load_dword s81, s[46:47], 2304\n"
    "s_cmp_eq_u64 s[80:81], 0\n"
    "s_cbranch_scc1 LseenS_%=\n"
    "s_nop 1\n"
    "s_cmp_eq_u64 s[80:81], 0\n"
    "s_cbranch_scc1 LseenS_%=\n"
    "s_nop 3\n"
    "s_load_dword s80, s[46:47], 384\n"
    "s_load_dword s81, s[46:47], 2432\n"
    "s_cmp_eq_u64 s[80:81], 0\n"
    "s_cbranch_scc1 LseenS_%=\n"
    "s_nop 1\n"
    "s_cmp_eq_u64 s[80:81], 0\n"
    "s_cbranch_scc1 LseenS_%=\n"
    "s_nop 3\n"
    "s_load_dword s80, s[46:47], 512\n"
    "s_load_dword s81, s[46:47], 2560\n"
    "s_cmp_eq_u64 s[80:81], 0\n"
    "s_cbranch_scc1 LseenS_%=\n"
    "s_nop 1\n"
    "s_cmp_eq_u64 s[80:81], 0\n"
    "s_cbranch_scc1 LseenS_%=\n"
    "s_nop 3\n"
    "s_load_dword s80, s[46:47], 640\n"
    "s_load_dword s81, s[46:47], 2688\n"
    "s_cmp_eq_u64 s[80:81], 0\n"
    "s_cbranch_scc1 LseenS_%=\n"
    "s_nop 1\n"
    "s_cmp_eq_u64 s[80:81], 0\n"
    "s_cbranch_scc1 LseenS_%=\n"
    "s_nop 3\n"
    "s_load_dword s80, s[46:47], 768\n"
    "s_load_dword s81, s[46:47], 2816\n"
    "s_cmp_eq_u64 s[80:81], 0\n"
    "s_cbranch_scc1 LseenS_%=\n"
    "s_nop 1\n"
    "s_cmp_eq_u64 s[80:81], 0\n"
    "s_cbranch_scc1 LseenS_%=\n"
    "s_nop 3\n"
    "s_load_dword s80, s[46:47], 896\n"
    "s_load_dword s81, s[46:47], 2944\n"
    "s_cmp_eq_u64 s[80:81], 0\n"
    "s_cbranch_scc1 LseenS_%=\n"
    "s_nop 1\n"
    "s_cmp_eq_u64 s[80:81], 0\n"
    "s_cbranch_scc1 LseenS_%=\n"
    "s_nop 3\n"
    "s_load_dword s80, s[46:47], 1024\n"
    "s_load_dword s81, s[46:47], 3072\n"
    "s_cmp_eq_u64 s[80:81], 0\n"
    "s_cbranch_scc1 LseenS_%=\n"
    "s_nop 1\n"
    "s_cmp_eq_u64 s[80:81], 0\n"
    "s_cbranch_scc1 LseenS_%=\n"
    "s_nop 3\n"
    "s_load_dword s80, s[46:47], 1152\n"
    "s_load_dword s81, s[46:47], 3200\n"
    "s_cmp_eq_u64 s[80:81], 0\n"
    "s_cbranch_scc1 LseenS_%=\n"
    "s_nop 1\n"
    "s_cmp_eq_u64 s[80:81], 0\n"
    "s_cbranch_scc1 LseenS_%=\n"
    "s_nop 3\n"
    "s_load_dword s80, s[46:47], 1280\n"
    "s_load_dword s81, s[46:47], 3328\n"
    "s_cmp_eq_u64 s[80:81], 0\n"
    "s_cbranch_scc1 LseenS_%=\n"
    "s_nop 1\n"
    "s_cmp_eq_u64 s[80:81], 0\n"
    "s_cbranch_scc1 LseenS_%=\n"
    "s_nop 3\n"
    "s_load_dword s80, s[46:47], 1408\n"
    "s_load_dword s81, s[46:47], 3456\n"
    "s_cmp_eq_u64 s[80:81], 0\n"
    "s_cbranch_scc1 LseenS_%=\n"
    "s_nop 1\n"
    "s_cmp_eq_u64 s[80:81], 0\n"
    "s_cbranch_scc1 LseenS_%=\n"
    "s_nop 3\n"
    "s_load_dword s80, s[46:47], 1536\n"
    "s_load_dword s81, s[46:47], 3584\n"
    "s_cmp_eq_u64 s[80:81], 0\n"
    "s_cbranch_scc1 LseenS_%=\n"
    "s_nop 1\n"
    "s_cmp_eq_u64 s[80:81], 0\n"
    "s_cbranch_scc1 LseenS_%=\n"
    "s_nop 3\n"
    "s_load_dword s80, s[46:47], 1664\n"
    "s_load_dword s81, s[46:47], 3712\n"
    "s_cmp_eq_u64 s[80:81], 0\n"
    "s_cbranch_scc1 LseenS_%=\n"
    "s_nop 1\n"
    "s_cmp_eq_u64 s[80:81], 0\n"
    "s_cbranch_scc1 LseenS_%=\n"
    "s_nop 3\n"
    "s_load_dword s80, s[46:47], 1792\n"
    "s_load_dword s81, s[46:47], 3840\n"
    "s_cmp_eq_u64 s[80:81], 0\n"
    "s_cbranch_scc1 LseenS_%=\n"
    "s_nop 1\n"
    "s_cmp_eq_u64 s[80:81], 0\n"
    "s_cbranch_scc1 LseenS_%=\n"
    "s_nop 3\n"
    "s_load_dword s80, s[46:47], 1920\n"
    "s_load_dword s81, s[46:47], 3968\n"
    "s_cmp_eq_u64 s[80:81], 0\n"
    "s_cbranch_scc1 LseenS_%=\n"
    "s_nop 1\n"
    "s_cmp_eq_u64 s[80:81], 0\n"
    "s_cbranch_scc1 LseenS_%=\n"
    "s_nop 3\n"
    "s_mov_b32 s59, 10\n"
    "Ldqs_%=:\n"
    "s_cmp_eq_u64 s[80:81], 0\n"
    "s_cbranch_scc1 LseenS_%=\n"
    "s_sub_u32 s59, s59, 1\n"
    "s_cbranch_scc0 Ldqs_%=\n"
    "s_add_u32 s78, %[own], 2048\n"
    "s_mov_b32 s59, 200\n"
    "Ldrs_%=:\n"
    "s_load_dword s80, s[46:47], %[own] glc\n"
    "s_load_dword s81, s[46:47], s78 glc\n"
    "s_waitcnt lgkmcnt(0)\n"
    "s_cmp_eq_u64 s[80:81], 0\n"
    "s_cbranch_scc1 LseenS_%=\n"
    "s_sub_u32 s59, s59, 1\n"
    "s_cbranch_scc0 Ldrs_%=\n"
    "LseenS_%=:\n"
    "s_setpc_b64 s[82:83]\n"
    ".rept 80\n"
    "s_nop 1\n"
    ".endr\n"
    "Lcs_%=:\n"
    "s_branch LloopG_%=\n"
    "LdoneS_%=:\n"
    "s_waitcnt vmcnt(0) lgkmcnt(0)\n"
    :
    : SCORE_IN
    :
      "memory", "vcc", "scc", "v26", "v27", "v28", "v29", "v30", "v31", "v32", "v33", "v34", "v35", "v36", "v37", "v38", "v39", "v40", "v41", "v42",
      "v43", "v44", "v45", "v46", "v47", "v48", "v49", "v50", "v51", "v52", "v53", "v54", "v55", "v56", "v57", "v58", "v59", "v60", "v61", "v62",
      "v63", "v64", "v65", "v66", "v67", "v68", "v69", "v70", "v71", "v72", "v73", "v74", "v75", "v76", "v77", "v78", "v79", "v80", "v81", "v82",
      "v83", "v84", "v85", "v86", "v87", "v88", "v89", "v90", "v91", "v92", "v93", "v94", "v95", "v96", "v97", "v98", "v99", "v100", "v101", "v102",
      "v103", "v104", "v105", "v106", "v107", "v108", "v109", "v110", "v111", "v112", "v113", "v114", "v115", "v116", "v117", "v118", "v119", "v120", "v121", "v122",
      "v123", "v124", "v125", "v126", "v127", "v128", "v129", "v130", "v131", "v132", "v133", "v134", "v135", "v136", "v137", "v138", "v139", "v140", "v141", "v142",
      "v143", "v144", "v145", "v146", "v147", "v148", "v149", "v150", "v151", "v152", "v153", "v154", "v155", "v156", "v157", "v158", "v159", "v160", "v161", "v162",
      "v163", "v164", "v165", "v166", "v167", "v168", "v169", "v170", "v171", "v172", "v173", "v174", "v175", "v176", "v177", "v178", "v179", "v180", "v181", "v182",
      "v183", "v184", "v185", "v186", "v187", "v188", "v189", "v190", "v191", "v192", "v193", "v194", "v195", "v196", "v197", "v198", "v199", "v200", "v201", "v202",
      "v203", "v204", "v205", "v206", "v207", "v208", "v209", "v210", "v211", "v212", "v213", "v214", "v215", "v216", "v217", "v218", "v219", "v220", "v221", "v222",
      "v223", "v224", "v225", "v226", "v227", "v228", "v229", "v230", "v231", "v232", "v233", "v234", "v235", "v236", "v237", "v238", "v239", "v240", "v241", "v242",
      "v243", "v244", "v245", "v246", "v247", "v248", "v249", "v250", "v251", "s38", "s39", "s40", "s41", "s42", "s43", "s44", "s45", "s46", "s47", "s48",
      "s49", "s50", "s51", "s52", "s53", "s54", "s55", "s56", "s57", "s58", "s59", "s60", "s61", "s62", "s63", "s64", "s65", "s66", "s67", "s68",
      "s69", "s70", "s71", "s72", "s73", "s74", "s75", "s76", "s77", "s78", "s79", "s80", "s81", "s82", "s83");
}

__device__ __forceinline__ void chain_role(char* ws, LAS char* lds, int r, int w, int lane, int tid, u32 d0, u32 d1, rsrc_t rs, v2u ep) {
  const int h = r >> 2, q = r & 3, cg = r < 56 ? r : r - 8;
  const int g = lane >> 4, hh = (lane >> 3) & 1, m = lane & 7, kap = 2 * (g & 1) + (g >> 1), a = w - 4;
  const u32 lane16 = lane * 16, voff16 = (w < 4 ? 3 + w : a) * 1024 + lane16, idx = hh ? 0xEEEEu : 0x4444u;
  const u32 trrd = g * 320 + (8 * ((lane & 3) >> 1) + ((lane & 15) >> 2)) * 16 + (lane & 1) * 8;
  const u32 rd = O_WF + (448 + hh * 28 + (cg >> 1)) * 14336 + (g * 16 + 8 * (cg & 1) + m) * 16;
  u32 ra, rb, rc, comb4 = r * 128 + m * 16 + kap * 4, flag, opf, oby = 0;
  if (w < 4) {
    const int gr = (lane >> 2) & 3, rr = lane & 3;
    ra = ((4 * w + hh) * 4 + g) * 128 + m * 16;
    rb = w == 0 ? (r & 1) * 4096 : rd + 4 * w * 28 * 14336;
    rc = ((1 + (rr & 1)) << (2 * (2 * (gr & 1) + (gr >> 1)))) | ((rr >> 1) << 8) | ((rr == 0) << 9) | ((rr == 1) << 10);
    flag = lane < 47 ? O_ZFLAG + lane * 128 : O_GFLAG + (56 + (((4 * w + rr) * 14 + (cg >> 2)) >> 2)) * 128;
    opf = O_VX + cg * 16384 + w * 2048;
  } else {
    ra = a * 640 + (lane & 15) * 16 + kap * 4;
    rb = O_WF + (h * 28 + 7 * q + 2 * a) * 14336 + lane16;
    rc = a < 3 ? trrd : O_CFLAG + r * 128;
    flag = lane16 + 7 * q * 256;
    oby = O_KM + h * 16384 + 7 * q * 256;
    opf = a == 2 ? O_W + (r >> 2) * 16384 + (r & 3) * 2048 : ((u32)(size_t)ws + O_P0) ^ ((u32)(size_t)ws + O_P1 + 0x4000);
  }
  const u32 cpy = (r & 1) * 4 * MB, rep = (u32)(r + 2) >> 6;
  opf = __builtin_amdgcn_readfirstlane(opf);
  oby = __builtin_amdgcn_readfirstlane(oby);
  const u32 own = ((r >> 1) & 15) * 128;
  const u32 osg = __builtin_amdgcn_readfirstlane((w ? O_P5 : O_P1 + 4 * MB + 0x4000) + (cpy >> 10));
  const u32 go = O_GO + r * 128;
  const u64 epoch = ((u64)ep.y << 32 | ep.x) >> 8;
  const int hb = w == 7 ? lane & 1 : w == 0 ? 2 + (lane & 1) : 4 * w + (lane & 3);
  const bool vside = w < 4 || (w == 7 && (lane & 2));
  const u32 wfl = O_PREP + gemm_wg(vside ? 56 + ((hb * 14 + (cg >> 2)) >> 2) : (h * 14 + ((7 * q + 2 * a + (a < 3 ? lane & 1 : 0)) >> 1)) >> 2) * 128;
#define CHAIN_IN [voff16] "v"(voff16), [lane16] "v"(lane16), [comb4] "v"(comb4), [ra] "v"(ra), [rb] "v"(rb), [rc] "v"(rc), [rd] "v"(rd), [wfl] "v"(wfl), \
      [flag] "v"(flag), [idx] "v"(idx), [base] "s"(ws), \
      [osg] "s"(osg), [opf] "s"(opf), [oby] "s"(oby), [d0] "s"(d0), [d1] "s"(d1), [wave] "s"(w), [cpy] "s"(cpy), [rep] "s"(rep), [go] "s"(go), [own] "s"(own), [epoch] "s"(epoch)
  asm volatile(
    "v_add_u32 v183, 0x400000, %[comb4]\n"
    "v_min_u32 v30, 7152, %[voff16]\n"
    "s_mov_b64 s[40:41], %[base]\n"
    "s_add_u32 s40, s40, 8425472\n"
    "s_addc_u32 s41, s41, 0\n"
    "s_mov_b64 s[42:43], %[base]\n"
    "s_add_u32 s42, s42, 32768\n"
    "s_addc_u32 s43, s43, 0\n"
    "s_mov_b64 s[44:45], %[base]\n"
    "s_add_u32 s44, s44, 16384\n"
    "s_addc_u32 s45, s45, 0\n"
    "s_mov_b64 s[46:47], %[base]\n"
    "s_add_u32 s46, s46, 2097152\n"
    "s_addc_u32 s47, s47, 0\n"
    "s_mov_b64 s[48:49], s[46:47]\n"
    "s_xor_b32 s48, s48, 0x2000\n"
    "s_mov_b64 s[38:39], %[base]\n"
    "s_add_u32 s38, s38, %[osg]\n"
    "s_addc_u32 s39, s39, 0\n"
    "s_mov_b32 s56, 0\n"
    "s_mov_b32 s57, 1\n"
    "s_mov_b32 s58, 0x80000\n"
    "s_mov_b32 s66, 0\n"
    "s_mov_b32 s60, 0x00ff00ff\n"
    "s_mov_b32 s61, 0x00ff00ff\n"
    "s_mov_b32 s71, 0\n"
    "s_mov_b32 s72, 0\n"
    "s_mov_b32 s73, 0\n"
    "s_mov_b32 s74, 0\n"
    "s_mov_b32 s76, -1\n"
    "s_mov_b32 s77, 0xffff\n"
    "s_mov_b32 s68, 0xffff0000\n"
    "s_mov_b32 s69, 0xff7fffff\n"
    "s_mov_b32 s70, 0x2f800000\n"
    "s_mov_b32 s75, 0x20000000\n"
    "v_mov_b32 v172, 0\n"
    "v_mov_b32 v173, 0\n"
    "v_mov_b32 v174, 0\n"
    "v_mov_b32 v175, 0\n"
    "v_mov_b32 v176, 0\n"
    "v_mov_b32 v177, 0\n"
    "v_mov_b32 v178, 0\n"
    "v_mov_b32 v179, 0\n"
    "s_cmp_eq_u32 %[wave], 0\n"
    "s_cbranch_scc1 LB0_%=\n"
    "s_cmp_eq_u32 %[wave], 4\n"
    "s_cbranch_scc1 LA0_%=\n"
    "s_cmp_eq_u32 %[wave], 5\n"
    "s_cbranch_scc1 LA1_%=\n"
    "s_cmp_eq_u32 %[wave], 6\n"
    "s_cbranch_scc1 LA2_%=\n"
    "s_cmp_eq_u32 %[wave], 7\n"
    "s_cbranch_scc1 LA3_%=\n"
    "v_add_u32 v29, %[cpy], %[ra]\n"
    "v_mov_b32 v188, %[rb]\n"
    "v_add_u32 v189, 802816, %[rb]\n"
    "s_lshl_b64 s[78:79], %[epoch], 8\n"
    "Lw4B_%=:\n"
    "global_load_dwordx2 v[184:185], %[wfl], %[base] offset:-512 sc1\n"
    "s_waitcnt vmcnt(0)\n"
    "Lwc4B_%=:\n"
    "v_xor_b32 v184, s78, v184\n"
    "v_xor_b32 v185, s79, v185\n"
    "v_subrev_u32 v184, 3, v184\n"
    "v_lshrrev_b32 v184, 3, v184\n"
    "v_or_b32 v184, v184, v185\n"
    "v_cmp_ne_u32 vcc, 0, v184\n"
    "s_cbranch_vccz Lwk4B_%=\n"
    "s_cmp_eq_u32 s58, 0\n"
    "s_cbranch_scc1 Lwk4B_%=\n"
    "s_sub_u32 s58, s58, 1\n"
    "s_sleep 6\n"
    "s_branch Lw4B_%=\n"
    "Lwk4B_%=:\n"
    "v_add_u32 v186, 4096, v188\n"
    "global_load_dwordx4 v[48:51], v186, %[base] offset:0 sc1\n"
    "global_load_dwordx4 v[52:55], v186, %[base] offset:1024 sc1\n"
    "global_load_dwordx4 v[56:59], v186, %[base] offset:2048 sc1\n"
    "v_add_u32 v186, 4096, v189\n"
    "global_load_dwordx4 v[104:107], v186, %[base] offset:0 sc1\n"
    "global_load_dwordx4 v[108:111], v186, %[base] offset:1024 sc1\n"
    "global_load_dwordx4 v[112:115], v186, %[base] offset:2048 sc1\n"
    "s_waitcnt vmcnt(2)\n"
    "global_load_dwordx2 v[184:185], %[wfl], %[base] offset:0 sc1\n"
    "v_add_u32 v186, 11264, v188\n"
    "global_load_dwordx4 v[76:79], v186, %[base] offset:0 sc1\n"
    "global_load_dwordx4 v[80:83], v186, %[base] offset:1024 sc1\n"
    "global_load_dwordx4 v[84:87], v186, %[base] offset:2048 sc1\n"
    "v_add_u32 v186, 11264, v189\n"
    "global_load_dwordx4 v[132:135], v186, %[base] offset:0 sc1\n"
    "global_load_dwordx4 v[136:139], v186, %[base] offset:1024 sc1\n"
    "global_load_dwordx4 v[140:143], v186, %[base] offset:2048 sc1\n"
    "s_waitcnt vmcnt(6)\n"
    "s_branch Lwc0B_%=\n"
    "Lw0B_%=:\n"
    "global_load_dwordx2 v[184:185], %[wfl], %[base] offset:0 sc1\n"
    "s_waitcnt vmcnt(0)\n"
    "Lwc0B_%=:\n"
    "v_xor_b32 v184, s78, v184\n"
    "v_xor_b32 v185, s79, v185\n"
    "v_subrev_u32 v184, 4, v184\n"
    "v_lshrrev_b32 v184, 3, v184\n"
    "v_or_b32 v184, v184, v185\n"
    "v_cmp_ne_u32 vcc, 0, v184\n"
    "s_cbranch_vccz Lwk0B_%=\n"
    "s_cmp_eq_u32 s58, 0\n"
    "s_cbranch_scc1 Lwk0B_%=\n"
    "s_sub_u32 s58, s58, 1\n"
    "s_sleep 6\n"
    "s_branch Lw0B_%=\n"
    "Lwk0B_%=:\n"
    "v_add_u32 v186, 0, v188\n"
    "global_load_dwordx4 v[32:35], v186, %[base] offset:0 sc1\n"
    "global_load_dwordx4 v[36:39], v186, %[base] offset:1024 sc1\n"
    "global_load_dwordx4 v[40:43], v186, %[base] offset:2048 sc1\n"
    "global_load_dwordx4 v[44:47], v186, %[base] offset:3072 sc1\n"
    "v_add_u32 v186, 0, v189\n"
    "global_load_dwordx4 v[88:91], v186, %[base] offset:0 sc1\n"
    "global_load_dwordx4 v[92:95], v186, %[base] offset:1024 sc1\n"
    "global_load_dwordx4 v[96:99], v186, %[base] offset:2048 sc1\n"
    "global_load_dwordx4 v[100:103], v186, %[base] offset:3072 sc1\n"
    "v_add_u32 v186, 7168, v188\n"
    "global_load_dwordx4 v[60:63], v186, %[base] offset:0 sc1\n"
    "global_load_dwordx4 v[64:67], v186, %[base] offset:1024 sc1\n"
    "global_load_dwordx4 v[68:71], v186, %[base] offset:2048 sc1\n"
    "global_load_dwordx4 v[72:75], v186, %[base] offset:3072 sc1\n"
    "v_add_u32 v186, 7168, v189\n"
    "global_load_dwordx4 v[116:119], v186, %[base] offset:0 sc1\n"
    "global_load_dwordx4 v[120:123], v186, %[base] offset:1024 sc1\n"
    "global_load_dwordx4 v[124:127], v186, %[base] offset:2048 sc1\n"
    "global_load_dwordx4 v[128:131], v186, %[base] offset:3072 sc1\n"
    "v_bfe_u32 v184, %[rc], 0, 2\n"
    "v_mul_u32_u24 v185, 0x3f80, v184\n"
    "v_cmp_eq_u32 vcc, 2, v184\n"
    "v_mov_b32 v186, 0x3f800000\n"
    "s_nop 0\n"
    "v_cndmask_b32 v185, v185, v186, vcc\n"
    "v_bfe_i32 v186, %[rc], 8, 1\n"
    "v_and_b32 v228, v185, v186\n"
    "v_xor_b32 v224, v185, v228\n"
    "v_bfe_u32 v184, %[rc], 2, 2\n"
    "v_mul_u32_u24 v185, 0x3f80, v184\n"
    "v_cmp_eq_u32 vcc, 2, v184\n"
    "v_mov_b32 v186, 0x3f800000\n"
    "s_nop 0\n"
    "v_cndmask_b32 v185, v185, v186, vcc\n"
    "v_bfe_i32 v186, %[rc], 8, 1\n"
    "v_and_b32 v229, v185, v186\n"
    "v_xor_b32 v225, v185, v229\n"
    "v_bfe_u32 v184, %[rc], 4, 2\n"
    "v_mul_u32_u24 v185, 0x3f80, v184\n"
    "v_cmp_eq_u32 vcc, 2, v184\n"
    "v_mov_b32 v186, 0x3f800000\n"
    "s_nop 0\n"
    "v_cndmask_b32 v185, v185, v186, vcc\n"
    "v_bfe_i32 v186, %[rc], 8, 1\n"
    "v_and_b32 v230, v185, v186\n"
    "v_xor_b32 v226, v185, v230\n"
    "v_bfe_u32 v184, %[rc], 6, 2\n"
    "v_mul_u32_u24 v185, 0x3f80, v184\n"
    "v_cmp_eq_u32 vcc, 2, v184\n"
    "v_mov_b32 v186, 0x3f800000\n"
    "s_nop 0\n"
    "v_cndmask_b32 v185, v185, v186, vcc\n"
    "v_bfe_i32 v186, %[rc], 8, 1\n"
    "v_and_b32 v231, v185, v186\n"
    "v_xor_b32 v227, v185, v231\n"
    "v_bfe_i32 v184, %[rc], 9, 1\n"
    "v_bfe_i32 v185, %[rc], 10, 1\n"
    "v_and_b32 v232, 0x3f803f80, v184\n"
    "v_and_b32 v236, 0x3f803f80, v185\n"
    "v_and_b32 v233, 0x3f803f80, v184\n"
    "v_and_b32 v237, 0x3f803f80, v185\n"
    "v_and_b32 v234, 0x3f803f80, v184\n"
    "v_and_b32 v238, 0x3f803f80, v185\n"
    "v_and_b32 v235, 0x3f803f80, v184\n"
    "v_and_b32 v239, 0x3f803f80, v185\n"
    "v_mov_b32 v240, v232\n"
    "v_mov_b32 v241, v236\n"
    "v_mov_b32 v243, 0\n"
    "v_mov_b32 v244, 0\n"
    "s_lshl_b32 s59, %[wave], 9\n"
    "v_lshrrev_b32 v181, 1, %[lane16]\n"
    "v_add_u32 v181, s59, v181\n"
    "v_lshrrev_b32 v182, 2, %[lane16]\n"
    "v_add_u32 v182, s59, v182\n"
    "s_mov_b64 s[50:51], %[base]\n"
    "s_add_u32 s50, s50, %[opf]\n"
    "s_addc_u32 s51, s51, 0\n"
    "v_mov_b32 v186, %[go]\n"
    "LgoB_%=:\n"
    "global_load_dwordx2 v[184:185], v186, %[base] sc1\n"
    "s_waitcnt vmcnt(0)\n"
    "v_readfirstlane_b32 s78, v184\n"
    "v_readfirstlane_b32 s79, v185\n"
    "s_nop 1\n"
    "s_lshr_b64 s[80:81], s[78:79], 8\n"
    "s_cmp_eq_u64 s[80:81], %[epoch]\n"
    "s_cbranch_scc1 LgokB_%=\n"
    "s_cmp_eq_u32 s58, 0\n"
    "s_cbranch_scc1 LgokB_%=\n"
    "s_sub_u32 s58, s58, 1\n"
    "s_sleep 2\n"
    "s_branch LgoB_%=\n"
    "LgokB_%=:\n"
    "s_bfe_u32 s79, s78, 0x20002\n"
    "s_and_b32 s78, s78, 3\n"
    "s_lshl_b32 s78, s78, 13\n"
    "s_lshl_b32 s79, s79, 13\n"
    "s_xor_b32 s42, s42, s78\n"
    "s_xor_b32 s44, s44, s78\n"
    "s_xor_b32 s46, s46, s79\n"
    "s_xor_b32 s48, s48, s79\n"
    "s_xor_b32 s38, s38, s79\n"
    "s_and_b32 s78, s78, 0x2000\n"
    "s_xor_b32 s50, s50, s78\n"
    "s_branch Lfl0B_%=\n"
    "Lflgo0B_%=:\n"
    "LloopB_%=:\n"
    "s_barrier\n"
    "LinB_%=:\n"
    "s_nop 4\n"
    "global_load_dwordx4 v[184:187], v30, s[40:41] sc1\n"
    "s_nop 11\n"
    "global_load_dwordx4 v[200:203], %[lane16], s[50:51] sc1\n"
    "global_load_dwordx4 v[204:207], %[lane16], s[50:51] offset:1024 sc1\n"
    "s_waitcnt vmcnt(2)\n"
    "ds_write_b128 %[voff16], v[184:187] offset:0\n"
    "v_max3_u32 v188, v184, v185, v186\n"
    "v_max_u32 v188, v188, v187\n"
    "v_cmp_eq_u32 vcc, -1, v188\n"
    "s_barrier\n"
    "s_cbranch_vccnz LmemretryB_%=\n"
    "LmemgoB_%=:\n"
    "v_add_u32 v30, %[cpy], %[voff16]\n"
    "s_waitcnt lgkmcnt(0)\n"
    "s_barrier\n"
    "s_nop 15\n"
    "s_nop 7\n"
    "ds_read_b128 v[144:147], %[lane16] offset:0\n"
    "ds_read_b128 v[148:151], %[lane16] offset:1024\n"
    "ds_read_b128 v[152:155], %[lane16] offset:2048\n"
    "ds_read_b128 v[156:159], %[lane16] offset:3072\n"
    "ds_read_b128 v[160:163], %[lane16] offset:4096\n"
    "ds_read_b128 v[164:167], %[lane16] offset:5120\n"
    "ds_read_b128 v[168:171], %[lane16] offset:6144\n"
    "s_cmp_eq_u32 s56, 63\n"
    "s_waitcnt lgkmcnt(0)\n"
    "s_barrier\n"
    "s_cbranch_scc1 LlastB_%=\n"
    "s_cmp_lg_u32 %[rep], 0\n"
    "s_cbranch_scc1 LrbdB_%=\n"
    "v_smfmac_f32_16x16x64_bf16 v[172:175], v[144:147], v[32:39], %[idx]\n"
    "v_smfmac_f32_16x16x64_bf16 v[172:175], v[148:151], v[40:47], %[idx]\n"
    "v_smfmac_f32_16x16x64_bf16 v[172:175], v[152:155], v[48:55], %[idx]\n"
    "v_smfmac_f32_16x16x64_bf16 v[172:175], v[156:159], v[56:63], %[idx]\n"
    "v_smfmac_f32_16x16x64_bf16 v[172:175], v[160:163], v[64:71], %[idx]\n"
    "v_smfmac_f32_16x16x64_bf16 v[172:175], v[164:167], v[72:79], %[idx]\n"
    "v_smfmac_f32_16x16x64_bf16 v[172:175], v[168:171], v[80:87], %[idx]\n"
    "s_nop 7\n"
    "v_permlane32_swap_b32 v172, v174\n"
    "v_permlane32_swap_b32 v173, v175\n"
    "v_add_f32 v172, v172, v174\n"
    "v_add_f32 v173, v173, v175\n"
    "v_cvt_pk_bf16_f32 v242, v172, v173\n"
    "ds_write_b32 v182, v242 offset:12288\n"
    "v_mov_b32 v172, 0\n"
    "v_mov_b32 v173, 0\n"
    "v_mov_b32 v174, 0\n"
    "v_mov_b32 v175, 0\n"
    "s_nop 3\n"
    "global_load_dwordx4 v[208:211], v29, s[44:45] sc1\n"
    "global_load_dwordx4 v[212:215], v29, s[44:45] offset:1024 sc1\n"
    "s_setprio 3\n"
    "s_barrier\n"
    "global_load_dwordx4 v[216:219], v29, s[46:47] sc1\n"
    "global_load_dwordx4 v[220:223], v29, s[46:47] offset:1024 sc1\n"
    "v_smfmac_f32_16x16x64_bf16 v[176:179], v[144:147], v[88:95], %[idx]\n"
    "v_smfmac_f32_16x16x64_bf16 v[176:179], v[148:151], v[96:103], %[idx]\n"
    "v_smfmac_f32_16x16x64_bf16 v[176:179], v[152:155], v[104:111], %[idx]\n"
    "v_smfmac_f32_16x16x64_bf16 v[176:179], v[156:159], v[112:119], %[idx]\n"
    "v_smfmac_f32_16x16x64_bf16 v[176:179], v[160:163], v[120:127], %[idx]\n"
    "v_smfmac_f32_16x16x64_bf16 v[176:179], v[164:167], v[128:135], %[idx]\n"
    "v_smfmac_f32_16x16x64_bf16 v[176:179], v[168:171], v[136:143], %[idx]\n"
    "s_nop 7\n"
    "v_permlane32_swap_b32 v176, v178\n"
    "v_permlane32_swap_b32 v177, v179\n"
    "v_add_f32 v176, v176, v178\n"
    "v_add_f32 v177, v177, v179\n"
    "v_cvt_pk_bf16_f32 v245, v176, v177\n"
    "ds_write_b32 v182, v245 offset:12544\n"
    "v_mov_b32 v176, 0\n"
    "v_mov_b32 v177, 0\n"
    "v_mov_b32 v178, 0\n"
    "v_mov_b32 v179, 0\n"
    "s_waitcnt vmcnt(2)\n"
    "LxsB_%=:\n"
    "v_max3_u32 v184, v208, v209, v210\n"
    "v_max3_u32 v184, v184, v211, v212\n"
    "v_max3_u32 v184, v184, v213, v214\n"
    "v_max_u32 v184, v184, v215\n"
    "v_cmp_eq_u32 vcc, -1, v184\n"
    "v_mfma_f32_16x16x32_bf16 v[248:251], v[232:235], v[208:211], 0\n"
    "v_mfma_f32_16x16x32_bf16 v[248:251], v[236:239], v[212:215], v[248:251]\n"
    "v_mfma_f32_16x16x32_bf16 v[144:147], v[200:203], v[208:211], 0\n"
    "v_mfma_f32_16x16x32_bf16 v[148:151], v[204:207], v[212:215], 0\n"
    "s_cbranch_vccnz LxsretryB_%=\n"
    "LxsgoB_%=:\n"
    "s_waitcnt vmcnt(1)\n"
    "LsmB_%=:\n"
    "v_mfma_f32_16x16x32_bf16 v[164:167], v[224:227], v[216:219], 0\n"
    "s_waitcnt vmcnt(0)\n"
    "v_mfma_f32_16x16x32_bf16 v[164:167], v[228:231], v[220:223], v[164:167]\n"
    "v_max3_u32 v188, v216, v217, v218\n"
    "v_max3_u32 v188, v188, v219, v220\n"
    "v_max3_u32 v188, v188, v221, v222\n"
    "v_max_u32 v188, v188, v223\n"
    "v_cmp_eq_u32 vcc, -1, v188\n"
    "s_nop 2\n"
    "v_exp_f32 v184, v164\n"
    "v_exp_f32 v185, v165\n"
    "v_exp_f32 v186, v166\n"
    "v_exp_f32 v187, v167\n"
    "s_cbranch_vccnz LsmretryB_%=\n"
    "LsmgoB_%=:\n"
    "v_cvt_pk_bf16_f32 v246, v184, v185\n"
    "v_cvt_pk_bf16_f32 v247, v186, v187\n"
    "s_nop 1\n"
    "LtailB_%=:\n"
    "v_mfma_f32_16x16x16_bf16 v[152:155], v[240:241], v[246:247], v[248:251]\n"
    "v_mfma_f32_16x16x16_bf16 v[156:159], v[242:243], v[246:247], v[144:147]\n"
    "v_mfma_f32_16x16x16_bf16 v[160:163], v[244:245], v[246:247], v[148:151]\n"
    "s_nop 5\n"
    "v_rcp_f32 v184, v152\n"
    "v_rcp_f32 v185, v153\n"
    "v_mul_f32 v156, v156, v184\n"
    "v_mul_f32 v158, v158, v184\n"
    "v_mul_f32 v157, v157, v184\n"
    "v_mul_f32 v159, v159, v184\n"
    "v_fmac_f32 v156, v160, v185\n"
    "v_fmac_f32 v158, v162, v185\n"
    "v_fmac_f32 v157, v161, v185\n"
    "v_fmac_f32 v159, v163, v185\n"
    "v_permlane32_swap_b32 v156, v158\n"
    "v_subrev_u32 v188, s70, v152\n"
    "v_permlane32_swap_b32 v157, v159\n"
    "v_add_f32_dpp v186, v158, v156 row_ror:8 row_mask:0xf bank_mask:0xf\n"
    "v_subrev_u32 v189, s70, v153\n"
    "v_add_f32_dpp v187, v159, v157 row_ror:8 row_mask:0xf bank_mask:0xf\n"
    "ds_write_b64 v181, v[186:187] offset:10240\n"
    "v_max_u32 v188, v188, v189\n"
    "v_cmp_lt_u32 vcc, s75, v188\n"
    "s_cbranch_vccnz LslowB_%=\n"
    "LokB_%=:\n"
    "s_waitcnt lgkmcnt(0)\n"
    "s_barrier\n"
    "s_setprio 0\n"
    "s_mov_b32 s66, 0\n"
    "s_add_u32 s56, s56, 1\n"
    "s_mov_b64 s[40:41], s[42:43]\n"
    "s_and_b32 s59, s56, 15\n"
    "s_cselect_b32 s59, 0, 0x4000\n"
    "s_add_u32 s44, s44, 0x8000\n"
    "s_xor_b32 s44, s44, s59\n"
    "s_add_u32 s46, s46, 0x8000\n"
    "s_xor_b32 s46, s46, s59\n"
    "s_add_u32 s48, s48, 0x8000\n"
    "s_xor_b32 s48, s48, s59\n"
    "s_add_u32 s59, s56, 1\n"
    "s_and_b32 s59, s59, 15\n"
    "s_cselect_b32 s59, 0, 0x4000\n"
    "s_add_u32 s42, s42, 0x8000\n"
    "s_xor_b32 s42, s42, s59\n"
    "s_xor_b32 s50, s50, 0x2000\n"
    "s_add_u32 s50, s50, 2097152\n"
    "s_addc_u32 s51, s51, 0\n"
    "s_branch LloopB_%=\n"
    "LlastB_%=:\n"
    "v_smfmac_f32_16x16x64_bf16 v[172:175], v[144:147], v[32:39], %[idx]\n"
    "v_smfmac_f32_16x16x64_bf16 v[172:175], v[148:151], v[40:47], %[idx]\n"
    "v_smfmac_f32_16x16x64_bf16 v[172:175], v[152:155], v[48:55], %[idx]\n"
    "v_smfmac_f32_16x16x64_bf16 v[172:175], v[156:159], v[56:63], %[idx]\n"
    "v_smfmac_f32_16x16x64_bf16 v[172:175], v[160:163], v[64:71], %[idx]\n"
    "v_smfmac_f32_16x16x64_bf16 v[172:175], v[164:167], v[72:79], %[idx]\n"
    "v_smfmac_f32_16x16x64_bf16 v[172:175], v[168:171], v[80:87], %[idx]\n"
    "s_nop 7\n"
    "v_permlane32_swap_b32 v172, v174\n"
    "v_permlane32_swap_b32 v173, v175\n"
    "v_add_f32 v172, v172, v174\n"
    "v_add_f32 v173, v173, v175\n"
    "v_cvt_pk_bf16_f32 v242, v172, v173\n"
    "ds_write_b32 v182, v242 offset:12288\n"
    "v_mov_b32 v172, 0\n"
    "v_mov_b32 v173, 0\n"
    "v_mov_b32 v174, 0\n"
    "v_mov_b32 v175, 0\n"
    "v_smfmac_f32_16x16x64_bf16 v[176:179], v[144:147], v[88:95], %[idx]\n"
    "v_smfmac_f32_16x16x64_bf16 v[176:179], v[148:151], v[96:103], %[idx]\n"
    "v_smfmac_f32_16x16x64_bf16 v[176:179], v[152:155], v[104:111], %[idx]\n"
    "v_smfmac_f32_16x16x64_bf16 v[176:179], v[156:159], v[112:119], %[idx]\n"
    "v_smfmac_f32_16x16x64_bf16 v[176:179], v[160:163], v[120:127], %[idx]\n"
    "v_smfmac_f32_16x16x64_bf16 v[176:179], v[164:167], v[128:135], %[idx]\n"
    "v_smfmac_f32_16x16x64_bf16 v[176:179], v[168:171], v[136:143], %[idx]\n"
    "s_nop 7\n"
    "v_permlane32_swap_b32 v176, v178\n"
    "v_permlane32_swap_b32 v177, v179\n"
    "v_add_f32 v176, v176, v178\n"
    "v_add_f32 v177, v177, v179\n"
    "v_cvt_pk_bf16_f32 v245, v176, v177\n"
    "ds_write_b32 v182, v245 offset:12544\n"
    "v_mov_b32 v176, 0\n"
    "v_mov_b32 v177, 0\n"
    "v_mov_b32 v178, 0\n"
    "v_mov_b32 v179, 0\n"
    "s_waitcnt lgkmcnt(0)\n"
    "s_barrier\n"
    "s_waitcnt vmcnt(0)\n"
    "s_barrier\n"
    "s_branch Lend_%=\n"
    "LmemretryB_%=:\n"
    "s_cmp_eq_u32 s58, 0\n"
    "s_cbranch_scc1 LmemgoB_%=\n"
    "s_sub_u32 s58, s58, 1\n"
    "s_cmp_eq_u32 %[rep], 0\n"
    "s_cbranch_scc1 LrsB_%=\n"
    "s_sleep 4\n"
    "LrsB_%=:\n"
    "global_load_dwordx4 v[184:187], v30, s[40:41] sc1\n"
    "s_waitcnt vmcnt(0)\n"
    "ds_write_b128 %[voff16], v[184:187] offset:0\n"
    "v_max3_u32 v188, v184, v185, v186\n"
    "v_max_u32 v188, v188, v187\n"
    "v_cmp_eq_u32 vcc, -1, v188\n"
    "s_cbranch_vccnz LmemretryB_%=\n"
    "s_branch LmemgoB_%=\n"
    "Lfl0B_%=:\n"
    "s_cmp_eq_u32 s58, 0\n"
    "s_cbranch_scc1 Lflgo0B_%=\n"
    "s_sub_u32 s58, s58, 1\n"
    "s_sleep 2\n"
    "global_load_dword v180, %[flag], %[base] sc1\n"
    "s_waitcnt vmcnt(0)\n"
    "v_cmp_gt_u32 vcc, s57, v180\n"
    "s_cbranch_vccnz Lfl0B_%=\n"
    "s_branch Lflgo0B_%=\n"
    "LrbdB_%=:\n"
    "s_barrier\n"
    "s_branch LokB_%=\n"
    "LxsretryB_%=:\n"
    "s_cmp_eq_u32 s58, 0\n"
    "s_cbranch_scc1 LxsgoB_%=\n"
    "s_sub_u32 s58, s58, 1\n"
    "global_load_dwordx4 v[208:211], v29, s[44:45] sc1\n"
    "global_load_dwordx4 v[212:215], v29, s[44:45] offset:1024 sc1\n"
    "s_waitcnt vmcnt(0)\n"
    "s_branch LxsB_%=\n"
    "LsmretryB_%=:\n"
    "s_cmp_eq_u32 s58, 0\n"
    "s_cbranch_scc1 LsmgoB_%=\n"
    "s_sub_u32 s58, s58, 1\n"
    "global_load_dwordx4 v[216:219], v29, s[46:47] sc1\n"
    "global_load_dwordx4 v[220:223], v29, s[46:47] offset:1024 sc1\n"
    "s_waitcnt vmcnt(0)\n"
    "s_branch LsmB_%=\n"
    "LslowB_%=:\n"
    "s_cmp_lg_u32 s66, 0\n"
    "s_cbranch_scc1 LokB_%=\n"
    "LnumB_%=:\n"
    "global_load_dwordx4 v[208:211], %[ra], s[48:49] sc1\n"
    "global_load_dwordx4 v[212:215], %[ra], s[48:49] offset:1024 sc1\n"
    "s_waitcnt vmcnt(0)\n"
    "v_max3_u32 v184, v208, v209, v210\n"
    "v_max3_u32 v184, v184, v211, v212\n"
    "v_max3_u32 v184, v184, v213, v214\n"
    "v_max_u32 v184, v184, v215\n"
    "v_cmp_eq_u32 vcc, -1, v184\n"
    "s_cbranch_vccz LnumgoB_%=\n"
    "s_cmp_eq_u32 s58, 0\n"
    "s_cbranch_scc1 LnumgoB_%=\n"
    "s_sub_u32 s58, s58, 1\n"
    "s_branch LnumB_%=\n"
    "LnumgoB_%=:\n"
    "v_lshlrev_b32 v216, 16, v208\n"
    "v_and_b32 v217, s68, v208\n"
    "v_lshlrev_b32 v218, 16, v209\n"
    "v_and_b32 v219, s68, v209\n"
    "v_lshlrev_b32 v220, 16, v210\n"
    "v_and_b32 v221, s68, v210\n"
    "v_lshlrev_b32 v222, 16, v211\n"
    "v_and_b32 v223, s68, v211\n"
    "v_max3_f32 v184, v164, v165, s69\n"
    "v_max3_f32 v184, v184, v216, v217\n"
    "v_max3_f32 v184, v184, v218, v219\n"
    "v_max3_f32 v184, v184, v220, v221\n"
    "v_max3_f32 v184, v184, v222, v223\n"
    "v_mov_b32 v185, v184\n"
    "s_nop 1\n"
    "v_permlane16_swap_b32 v184, v185\n"
    "v_max_f32 v184, v184, v185\n"
    "v_mov_b32 v185, v184\n"
    "s_nop 1\n"
    "v_permlane32_swap_b32 v184, v185\n"
    "v_max_f32 v184, v184, v185\n"
    "v_sub_f32 v186, v164, v184\n"
    "v_sub_f32 v187, v165, v184\n"
    "v_sub_f32 v216, v216, v184\n"
    "v_sub_f32 v217, v217, v184\n"
    "v_sub_f32 v218, v218, v184\n"
    "v_sub_f32 v219, v219, v184\n"
    "v_sub_f32 v220, v220, v184\n"
    "v_sub_f32 v221, v221, v184\n"
    "v_sub_f32 v222, v222, v184\n"
    "v_sub_f32 v223, v223, v184\n"
    "v_exp_f32 v216, v216\n"
    "v_exp_f32 v217, v217\n"
    "v_exp_f32 v218, v218\n"
    "v_exp_f32 v219, v219\n"
    "v_exp_f32 v220, v220\n"
    "v_exp_f32 v221, v221\n"
    "v_exp_f32 v222, v222\n"
    "v_exp_f32 v223, v223\n"
    "v_exp_f32 v186, v186\n"
    "v_exp_f32 v187, v187\n"
    "v_cvt_pk_bf16_f32 v208, v216, v217\n"
    "v_cvt_pk_bf16_f32 v209, v218, v219\n"
    "v_cvt_pk_bf16_f32 v210, v220, v221\n"
    "v_cvt_pk_bf16_f32 v211, v222, v223\n"
    "v_cvt_pk_bf16_f32 v246, v186, v187\n"
    "v_lshlrev_b32 v216, 16, v212\n"
    "v_and_b32 v217, s68, v212\n"
    "v_lshlrev_b32 v218, 16, v213\n"
    "v_and_b32 v219, s68, v213\n"
    "v_lshlrev_b32 v220, 16, v214\n"
    "v_and_b32 v221, s68, v214\n"
    "v_lshlrev_b32 v222, 16, v215\n"
    "v_and_b32 v223, s68, v215\n"
    "v_max3_f32 v184, v166, v167, s69\n"
    "v_max3_f32 v184, v184, v216, v217\n"
    "v_max3_f32 v184, v184, v218, v219\n"
    "v_max3_f32 v184, v184, v220, v221\n"
    "v_max3_f32 v184, v184, v222, v223\n"
    "v_mov_b32 v185, v184\n"
    "s_nop 1\n"
    "v_permlane16_swap_b32 v184, v185\n"
    "v_max_f32 v184, v184, v185\n"
    "v_mov_b32 v185, v184\n"
    "s_nop 1\n"
    "v_permlane32_swap_b32 v184, v185\n"
    "v_max_f32 v184, v184, v185\n"
    "v_sub_f32 v186, v166, v184\n"
    "v_sub_f32 v187, v167, v184\n"
    "v_sub_f32 v216, v216, v184\n"
    "v_sub_f32 v217, v217, v184\n"
    "v_sub_f32 v218, v218, v184\n"
    "v_sub_f32 v219, v219, v184\n"
    "v_sub_f32 v220, v220, v184\n"
    "v_sub_f32 v221, v221, v184\n"
    "v_sub_f32 v222, v222, v184\n"
    "v_sub_f32 v223, v223, v184\n"
    "v_exp_f32 v216, v216\n"
    "v_exp_f32 v217, v217\n"
    "v_exp_f32 v218, v218\n"
    "v_exp_f32 v219, v219\n"
    "v_exp_f32 v220, v220\n"
    "v_exp_f32 v221, v221\n"
    "v_exp_f32 v222, v222\n"
    "v_exp_f32 v223, v223\n"
    "v_exp_f32 v186, v186\n"
    "v_exp_f32 v187, v187\n"
    "v_cvt_pk_bf16_f32 v212, v216, v217\n"
    "v_cvt_pk_bf16_f32 v213, v218, v219\n"
    "v_cvt_pk_bf16_f32 v214, v220, v221\n"
    "v_cvt_pk_bf16_f32 v215, v222, v223\n"
    "v_cvt_pk_bf16_f32 v247, v186, v187\n"
    "s_nop 1\n"
    "v_mfma_f32_16x16x32_bf16 v[248:251], v[232:235], v[208:211], 0\n"
    "v_mfma_f32_16x16x32_bf16 v[248:251], v[236:239], v[212:215], v[248:251]\n"
    "v_mfma_f32_16x16x32_bf16 v[144:147], v[200:203], v[208:211], 0\n"
    "v_mfma_f32_16x16x32_bf16 v[148:151], v[204:207], v[212:215], 0\n"
    "s_mov_b32 s66, 1\n"
    "s_nop 7\n"
    "s_branch LtailB_%=\n"
    "LB0_%=:\n"
    "v_add_u32 v29, %[cpy], %[ra]\n"
    "s_bfe_u32 s59, %[d0], 524312\n"
    "s_lshl_b32 s59, s59, 2\n"
    "s_getpc_b64 s[82:83]\n"
    "Lpcb0_%=:\n"
    "s_add_u32 s82, s82, Lc2_%=-Lpcb0_%=\n"
    "s_addc_u32 s83, s83, 0\n"
    "s_sub_u32 s82, s82, s59\n"
    "s_subb_u32 s83, s83, 0\n"
    "v_add_u32 v188, 802816, %[rd]\n"
    "s_lshl_b64 s[78:79], %[epoch], 8\n"
    "Lw4B0_%=:\n"
    "global_load_dwordx2 v[184:185], %[wfl], %[base] offset:-512 sc1\n"
    "s_waitcnt vmcnt(0)\n"
    "Lwc4B0_%=:\n"
    "v_xor_b32 v184, s78, v184\n"
    "v_xor_b32 v185, s79, v185\n"
    "v_subrev_u32 v184, 3, v184\n"
    "v_lshrrev_b32 v184, 3, v184\n"
    "v_or_b32 v184, v184, v185\n"
    "v_cmp_ne_u32 vcc, 0, v184\n"
    "s_cbranch_vccz Lwk4B0_%=\n"
    "s_cmp_eq_u32 s58, 0\n"
    "s_cbranch_scc1 Lwk4B0_%=\n"
    "s_sub_u32 s58, s58, 1\n"
    "s_sleep 6\n"
    "s_branch Lw4B0_%=\n"
    "Lwk4B0_%=:\n"
    "v_add_u32 v186, 4096, v188\n"
    "global_load_dwordx4 v[104:107], v186, %[base] offset:0 sc1\n"
    "global_load_dwordx4 v[108:111], v186, %[base] offset:1024 sc1\n"
    "global_load_dwordx4 v[112:115], v186, %[base] offset:2048 sc1\n"
    "s_waitcnt vmcnt(2)\n"
    "global_load_dwordx2 v[184:185], %[wfl], %[base] offset:0 sc1\n"
    "v_add_u32 v186, 11264, v188\n"
    "global_load_dwordx4 v[132:135], v186, %[base] offset:0 sc1\n"
    "global_load_dwordx4 v[136:139], v186, %[base] offset:1024 sc1\n"
    "global_load_dwordx4 v[140:143], v186, %[base] offset:2048 sc1\n"
    "s_waitcnt vmcnt(3)\n"
    "s_branch Lwc0B0_%=\n"
    "Lw0B0_%=:\n"
    "global_load_dwordx2 v[184:185], %[wfl], %[base] offset:0 sc1\n"
    "s_waitcnt vmcnt(0)\n"
    "Lwc0B0_%=:\n"
    "v_xor_b32 v184, s78, v184\n"
    "v_xor_b32 v185, s79, v185\n"
    "v_subrev_u32 v184, 4, v184\n"
    "v_lshrrev_b32 v184, 3, v184\n"
    "v_or_b32 v184, v184, v185\n"
    "v_cmp_ne_u32 vcc, 0, v184\n"
    "s_cbranch_vccz Lwk0B0_%=\n"
    "s_cmp_eq_u32 s58, 0\n"
    "s_cbranch_scc1 Lwk0B0_%=\n"
    "s_sub_u32 s58, s58, 1\n"
    "s_sleep 6\n"
    "s_branch Lw0B0_%=\n"
    "Lwk0B0_%=:\n"
    "v_add_u32 v186, 0, v188\n"
    "global_load_dwordx4 v[88:91], v186, %[base] offset:0 sc1\n"
    "global_load_dwordx4 v[92:95], v186, %[base] offset:1024 sc1\n"
    "global_load_dwordx4 v[96:99], v186, %[base] offset:2048 sc1\n"
    "global_load_dwordx4 v[100:103], v186, %[base] offset:3072 sc1\n"
    "v_add_u32 v186, 7168, v188\n"
    "global_load_dwordx4 v[116:119], v186, %[base] offset:0 sc1\n"
    "global_load_dwordx4 v[120:123], v186, %[base] offset:1024 sc1\n"
    "global_load_dwordx4 v[124:127], v186, %[base] offset:2048 sc1\n"
    "global_load_dwordx4 v[128:131], v186, %[base] offset:3072 sc1\n"
    "v_lshlrev_b32 v28, 3, %[lane16]\n"
    "v_or_b32 v28, %[cpy], v28\n"
    "s_lshr_b32 s59, %[cpy], 11\n"
    "s_sub_u32 s59, 4096, s59\n"
    "v_and_b32 v27, 16, %[lane16]\n"
    "v_lshlrev_b32 v27, 8, v27\n"
    "v_and_b32 v26, 0x1e0, %[lane16]\n"
    "v_lshl_add_u32 v27, v26, 2, v27\n"
    "v_add_u32 v27, s59, v27\n"
    "v_mov_b32 v26, 0\n"
    "v_bfe_u32 v184, %[rc], 0, 2\n"
    "v_mul_u32_u24 v185, 0x3f80, v184\n"
    "v_cmp_eq_u32 vcc, 2, v184\n"
    "v_mov_b32 v186, 0x3f800000\n"
    "s_nop 0\n"
    "v_cndmask_b32 v185, v185, v186, vcc\n"
    "v_bfe_i32 v186, %[rc], 8, 1\n"
    "v_and_b32 v228, v185, v186\n"
    "v_xor_b32 v224, v185, v228\n"
    "v_bfe_u32 v184, %[rc], 2, 2\n"
    "v_mul_u32_u24 v185, 0x3f80, v184\n"
    "v_cmp_eq_u32 vcc, 2, v184\n"
    "v_mov_b32 v186, 0x3f800000\n"
    "s_nop 0\n"
    "v_cndmask_b32 v185, v185, v186, vcc\n"
    "v_bfe_i32 v186, %[rc], 8, 1\n"
    "v_and_b32 v229, v185, v186\n"
    "v_xor_b32 v225, v185, v229\n"
    "v_bfe_u32 v184, %[rc], 4, 2\n"
    "v_mul_u32_u24 v185, 0x3f80, v184\n"
    "v_cmp_eq_u32 vcc, 2, v184\n"
    "v_mov_b32 v186, 0x3f800000\n"
    "s_nop 0\n"
    "v_cndmask_b32 v185, v185, v186, vcc\n"
    "v_bfe_i32 v186, %[rc], 8, 1\n"
    "v_and_b32 v230, v185, v186\n"
    "v_xor_b32 v226, v185, v230\n"
    "v_bfe_u32 v184, %[rc], 6, 2\n"
    "v_mul_u32_u24 v185, 0x3f80, v184\n"
    "v_cmp_eq_u32 vcc, 2, v184\n"
    "v_mov_b32 v186, 0x3f800000\n"
    "s_nop 0\n"
    "v_cndmask_b32 v185, v185, v186, vcc\n"
    "v_bfe_i32 v186, %[rc], 8, 1\n"
    "v_and_b32 v231, v185, v186\n"
    "v_xor_b32 v227, v185, v231\n"
    "v_bfe_i32 v184, %[rc], 9, 1\n"
    "v_bfe_i32 v185, %[rc], 10, 1\n"
    "v_and_b32 v232, 0x3f803f80, v184\n"
    "v_and_b32 v236, 0x3f803f80, v185\n"
    "v_and_b32 v233, 0x3f803f80, v184\n"
    "v_and_b32 v237, 0x3f803f80, v185\n"
    "v_and_b32 v234, 0x3f803f80, v184\n"
    "v_and_b32 v238, 0x3f803f80, v185\n"
    "v_and_b32 v235, 0x3f803f80, v184\n"
    "v_and_b32 v239, 0x3f803f80, v185\n"
    "v_mov_b32 v240, v232\n"
    "v_mov_b32 v241, v236\n"
    "v_mov_b32 v243, 0\n"
    "v_mov_b32 v244, 0\n"
    "s_lshl_b32 s59, %[wave], 9\n"
    "v_lshrrev_b32 v181, 1, %[lane16]\n"
    "v_add_u32 v181, s59, v181\n"
    "v_lshrrev_b32 v182, 2, %[lane16]\n"
    "v_add_u32 v182, s59, v182\n"
    "s_mov_b64 s[50:51], %[base]\n"
    "s_add_u32 s50, s50, %[opf]\n"
    "s_addc_u32 s51, s51, 0\n"
    "v_mov_b32 v186, %[go]\n"
    "LgoB0_%=:\n"
    "global_load_dwordx2 v[184:185], v186, %[base] sc1\n"
    "s_waitcnt vmcnt(0)\n"
    "v_readfirstlane_b32 s78, v184\n"
    "v_readfirstlane_b32 s79, v185\n"
    "s_nop 1\n"
    "s_lshr_b64 s[80:81], s[78:79], 8\n"
    "s_cmp_eq_u64 s[80:81], %[epoch]\n"
    "s_cbranch_scc1 LgokB0_%=\n"
    "s_cmp_eq_u32 s58, 0\n"
    "s_cbranch_scc1 LgokB0_%=\n"
    "s_sub_u32 s58, s58, 1\n"
    "s_sleep 2\n"
    "s_branch LgoB0_%=\n"
    "LgokB0_%=:\n"
    "s_bfe_u32 s79, s78, 0x20002\n"
    "s_and_b32 s78, s78, 3\n"
    "s_lshl_b32 s78, s78, 13\n"
    "s_lshl_b32 s79, s79, 13\n"
    "s_xor_b32 s42, s42, s78\n"
    "s_xor_b32 s44, s44, s78\n"
    "s_xor_b32 s46, s46, s79\n"
    "s_xor_b32 s48, s48, s79\n"
    "s_xor_b32 s38, s38, s79\n"
    "s_and_b32 s78, s78, 0x2000\n"
    "s_xor_b32 s50, s50, s78\n"
    "s_branch Lfl0B0_%=\n"
    "Lflgo0B0_%=:\n"
    "LloopB0_%=:\n"
    "s_barrier\n"
    "LinB0_%=:\n"
    "s_mov_b64 s[80:81], -1\n"
    "s_cmp_eq_u32 %[rep], 0\n"
    "s_nop 15\n"
    "global_load_dwordx4 v[200:203], %[lane16], s[50:51] sc1\n"
    "global_load_dwordx4 v[204:207], %[lane16], s[50:51] offset:1024 sc1\n"
    "s_barrier\n"
    "s_barrier\n"
    "s_cbranch_scc1 Lns2_%=\n"
    "s_mov_b32 exec_hi, 0\n"
    "global_store_dword v27, v26, s[38:39] offset:-4096 sc1\n"
    "s_mov_b64 exec, -1\n"
    "Lns2_%=:\n"
    "s_nop 7\n"
    "ds_read_b128 v[144:147], %[lane16] offset:0\n"
    "ds_read_b128 v[148:151], %[lane16] offset:1024\n"
    "ds_read_b128 v[152:155], %[lane16] offset:2048\n"
    "ds_read_b128 v[156:159], %[lane16] offset:3072\n"
    "ds_read_b128 v[160:163], %[lane16] offset:4096\n"
    "ds_read_b128 v[164:167], %[lane16] offset:5120\n"
    "ds_read_b128 v[168:171], %[lane16] offset:6144\n"
    "s_cmp_eq_u32 s56, 63\n"
    "s_waitcnt lgkmcnt(0)\n"
    "s_load_dword s80, s[38:39], 0\n"
    "s_load_dword s81, s[38:39], 2048\n"
    "s_nop 9\n"
    "s_load_dword s80, s[38:39], 128\n"
    "s_load_dword s81, s[38:39], 2176\n"
    "s_nop 9\n"
    "s_load_dword s80, s[38:39], 256\n"
    "s_load_dword s81, s[38:39], 2304\n"
    "s_nop 9\n"
    "s_load_dword s80, s[38:39], 384\n"
    "s_load_dword s81, s[38:39], 2432\n"
    "s_nop 9\n"
    "s_barrier\n"
    "s_cbranch_scc1 LlastB0_%=\n"
    "s_load_dword s80, s[38:39], 512\n"
    "s_load_dword s81, s[38:39], 2560\n"
    "s_nop 9\n"
    "s_load_dword s80, s[38:39], 640\n"
    "s_load_dword s81, s[38:39], 2688\n"
    "s_nop 9\n"
    "s_load_dword s80, s[38:39], 768\n"
    "s_load_dword s81, s[38:39], 2816\n"
    "s_nop 9\n"
    "s_load_dword s80, s[38:39], 896\n"
    "s_load_dword s81, s[38:39], 2944\n"
    "s_nop 9\n"
    "global_load_dwordx4 v[208:211], v29, s[44:45] sc1\n"
    "global_load_dwordx4 v[212:215], v29, s[44:45] offset:1024 sc1\n"
    "s_load_dword s80, s[38:39], 1024\n"
    "s_load_dword s81, s[38:39], 3072\n"
    "s_cmp_eq_u64 s[80:81], 0\n"
    "s_cbranch_scc1 Lseen2_%=\n"
    "s_nop 1\n"
    "s_cmp_eq_u64 s[80:81], 0\n"
    "s_cbranch_scc1 Lseen2_%=\n"
    "s_nop 3\n"
    "s_load_dword s80, s[38:39], 1152\n"
    "s_load_dword s81, s[38:39], 3200\n"
    "s_cmp_eq_u64 s[80:81], 0\n"
    "s_cbranch_scc1 Lseen2_%=\n"
    "s_nop 1\n"
    "s_cmp_eq_u64 s[80:81], 0\n"
    "s_cbranch_scc1 Lseen2_%=\n"
    "s_nop 3\n"
    "s_load_dword s80, s[38:39], 1280\n"
    "s_load_dword s81, s[38:39], 3328\n"
    "s_cmp_eq_u64 s[80:81], 0\n"
    "s_cbranch_scc1 Lseen2_%=\n"
    "s_nop 1\n"
    "s_cmp_eq_u64 s[80:81], 0\n"
    "s_cbranch_scc1 Lseen2_%=\n"
    "s_nop 3\n"
    "s_load_dword s80, s[38:39], 1408\n"
    "s_load_dword s81, s[38:39], 3456\n"
    "s_cmp_eq_u64 s[80:81], 0\n"
    "s_cbranch_scc1 Lseen2_%=\n"
    "s_nop 1\n"
    "s_cmp_eq_u64 s[80:81], 0\n"
    "s_cbranch_scc1 Lseen2_%=\n"
    "s_nop 3\n"
    "s_load_dword s80, s[38:39], 1536\n"
    "s_load_dword s81, s[38:39], 3584\n"
    "s_cmp_eq_u64 s[80:81], 0\n"
    "s_cbranch_scc1 Lseen2_%=\n"
    "s_nop 1\n"
    "s_cmp_eq_u64 s[80:81], 0\n"
    "s_cbranch_scc1 Lseen2_%=\n"
    "s_nop 3\n"
    "s_load_dword s80, s[38:39], 1664\n"
    "s_load_dword s81, s[38:39], 3712\n"
    "s_cmp_eq_u64 s[80:81], 0\n"
    "s_cbranch_scc1 Lseen2_%=\n"
    "s_nop 1\n"
    "s_cmp_eq_u64 s[80:81], 0\n"
    "s_cbranch_scc1 Lseen2_%=\n"
    "s_nop 3\n"
    "s_load_dword s80, s[38:39], 1792\n"
    "s_load_dword s81, s[38:39], 3840\n"
    "s_cmp_eq_u64 s[80:81], 0\n"
    "s_cbranch_scc1 Lseen2_%=\n"
    "s_nop 1\n"
    "s_cmp_eq_u64 s[80:81], 0\n"
    "s_cbranch_scc1 Lseen2_%=\n"
    "s_nop 3\n"
    "s_load_dword s80, s[38:39], 1920\n"
    "s_load_dword s81, s[38:39], 3968\n"
    "s_cmp_eq_u64 s[80:81], 0\n"
    "s_cbranch_scc1 Lseen2_%=\n"
    "s_nop 1\n"
    "s_cmp_eq_u64 s[80:81], 0\n"
    "s_cbranch_scc1 Lseen2_%=\n"
    "s_nop 3\n"
    "s_mov_b32 s59, 10\n"
    "Ldq2_%=:\n"
    "s_cmp_eq_u64 s[80:81], 0\n"
    "s_cbranch_scc1 Lseen2_%=\n"
    "s_sub_u32 s59, s59, 1\n"
    "s_cbranch_scc0 Ldq2_%=\n"
    "s_add_u32 s78, %[own], 2048\n"
    "s_mov_b32 s59, 200\n"
    "Ldr2_%=:\n"
    "s_load_dword s80, s[38:39], %[own] glc\n"
    "s_load_dword s81, s[38:39], s78 glc\n"
    "s_waitcnt lgkmcnt(0)\n"
    "s_cmp_eq_u64 s[80:81], 0\n"
    "s_cbranch_scc1 Lseen2_%=\n"
    "s_sub_u32 s59, s59, 1\n"
    "s_cbranch_scc0 Ldr2_%=\n"
    "Lseen2_%=:\n"
    "s_setprio 3\n"
    "s_setpc_b64 s[82:83]\n"
    ".rept 80\n"
    "s_nop 1\n"
    ".endr\n"
    "Lc2_%=:\n"
    "s_cmp_lg_u32 %[rep], 0\n"
    "s_cbranch_scc1 LrbdB0_%=\n"
    "s_barrier\n"
    "LrbedB0_%=:\n"
    "global_load_dwordx4 v[216:219], v29, s[46:47] sc1\n"
    "global_load_dwordx4 v[220:223], v29, s[46:47] offset:1024 sc1\n"
    "ds_read_b32 v242, v182 offset:12288\n"
    "v_smfmac_f32_16x16x64_bf16 v[176:179], v[144:147], v[88:95], %[idx]\n"
    "v_smfmac_f32_16x16x64_bf16 v[176:179], v[148:151], v[96:103], %[idx]\n"
    "v_smfmac_f32_16x16x64_bf16 v[176:179], v[152:155], v[104:111], %[idx]\n"
    "v_smfmac_f32_16x16x64_bf16 v[176:179], v[156:159], v[112:119], %[idx]\n"
    "v_smfmac_f32_16x16x64_bf16 v[176:179], v[160:163], v[120:127], %[idx]\n"
    "v_smfmac_f32_16x16x64_bf16 v[176:179], v[164:167], v[128:135], %[idx]\n"
    "v_smfmac_f32_16x16x64_bf16 v[176:179], v[168:171], v[136:143], %[idx]\n"
    "s_nop 7\n"
    "v_permlane32_swap_b32 v176, v178\n"
    "v_permlane32_swap_b32 v177, v179\n"
    "v_add_f32 v176, v176, v178\n"
    "v_add_f32 v177, v177, v179\n"
    "v_cvt_pk_bf16_f32 v245, v176, v177\n"
    "ds_write_b32 v182, v245 offset:12544\n"
    "v_mov_b32 v176, 0\n"
    "v_mov_b32 v177, 0\n"
    "v_mov_b32 v178, 0\n"
    "v_mov_b32 v179, 0\n"
    "s_waitcnt vmcnt(2)\n"
    "LxsB0_%=:\n"
    "v_max3_u32 v184, v208, v209, v210\n"
    "v_max3_u32 v184, v184, v211, v212\n"
    "v_max3_u32 v184, v184, v213, v214\n"
    "v_max_u32 v184, v184, v215\n"
    "v_cmp_eq_u32 vcc, -1, v184\n"
    "v_mfma_f32_16x16x32_bf16 v[248:251], v[232:235], v[208:211], 0\n"
    "v_mfma_f32_16x16x32_bf16 v[248:251], v[236:239], v[212:215], v[248:251]\n"
    "v_mfma_f32_16x16x32_bf16 v[144:147], v[200:203], v[208:211], 0\n"
    "v_mfma_f32_16x16x32_bf16 v[148:151], v[204:207], v[212:215], 0\n"
    "s_cbranch_vccnz LxsretryB0_%=\n"
    "LxsgoB0_%=:\n"
    "s_waitcnt vmcnt(1) lgkmcnt(0)\n"
    "LsmB0_%=:\n"
    "v_mfma_f32_16x16x32_bf16 v[164:167], v[224:227], v[216:219], 0\n"
    "s_waitcnt vmcnt(0)\n"
    "v_mfma_f32_16x16x32_bf16 v[164:167], v[228:231], v[220:223], v[164:167]\n"
    "v_max3_u32 v188, v216, v217, v218\n"
    "v_max3_u32 v188, v188, v219, v220\n"
    "v_max3_u32 v188, v188, v221, v222\n"
    "v_max_u32 v188, v188, v223\n"
    "v_cmp_eq_u32 vcc, -1, v188\n"
    "s_nop 2\n"
    "v_exp_f32 v184, v164\n"
    "v_exp_f32 v185, v165\n"
    "v_exp_f32 v186, v166\n"
    "v_exp_f32 v187, v167\n"
    "s_cbranch_vccnz LsmretryB0_%=\n"
    "LsmgoB0_%=:\n"
    "v_cvt_pk_bf16_f32 v246, v184, v185\n"
    "v_cvt_pk_bf16_f32 v247, v186, v187\n"
    "s_nop 1\n"
    "LtailB0_%=:\n"
    "v_mfma_f32_16x16x16_bf16 v[152:155], v[240:241], v[246:247], v[248:251]\n"
    "v_mfma_f32_16x16x16_bf16 v[156:159], v[242:243], v[246:247], v[144:147]\n"
    "v_mfma_f32_16x16x16_bf16 v[160:163], v[244:245], v[246:247], v[148:151]\n"
    "s_nop 5\n"
    "v_rcp_f32 v184, v152\n"
    "v_rcp_f32 v185, v153\n"
    "v_mul_f32 v156, v156, v184\n"
    "v_mul_f32 v158, v158, v184\n"
    "v_mul_f32 v157, v157, v184\n"
    "v_mul_f32 v159, v159, v184\n"
    "v_fmac_f32 v156, v160, v185\n"
    "v_fmac_f32 v158, v162, v185\n"
    "v_fmac_f32 v157, v161, v185\n"
    "v_fmac_f32 v159, v163, v185\n"
    "v_permlane32_swap_b32 v156, v158\n"
    "v_subrev_u32 v188, s70, v152\n"
    "v_permlane32_swap_b32 v157, v159\n"
    "v_add_f32_dpp v186, v158, v156 row_ror:8 row_mask:0xf bank_mask:0xf\n"
    "v_subrev_u32 v189, s70, v153\n"
    "v_add_f32_dpp v187, v159, v157 row_ror:8 row_mask:0xf bank_mask:0xf\n"
    "ds_write_b64 v181, v[186:187] offset:10240\n"
    "v_max_u32 v188, v188, v189\n"
    "v_cmp_lt_u32 vcc, s75, v188\n"
    "s_cbranch_vccnz LslowB0_%=\n"
    "LokB0_%=:\n"
    "s_waitcnt lgkmcnt(0)\n"
    "s_barrier\n"
    "s_setprio 0\n"
    "s_mov_b32 s66, 0\n"
    "s_add_u32 s56, s56, 1\n"
    "s_mov_b64 s[40:41], s[42:43]\n"
    "s_and_b32 s59, s56, 15\n"
    "s_cselect_b32 s59, 0, 0x4000\n"
    "s_add_u32 s44, s44, 0x8000\n"
    "s_xor_b32 s44, s44, s59\n"
    "s_add_u32 s46, s46, 0x8000\n"
    "s_xor_b32 s46, s46, s59\n"
    "s_add_u32 s48, s48, 0x8000\n"
    "s_xor_b32 s48, s48, s59\n"
    "s_add_u32 s38, s38, 0x8000\n"
    "s_xor_b32 s38, s38, s59\n"
    "s_add_u32 s59, s56, 1\n"
    "s_and_b32 s59, s59, 15\n"
    "s_cselect_b32 s59, 0, 0x4000\n"
    "s_add_u32 s42, s42, 0x8000\n"
    "s_xor_b32 s42, s42, s59\n"
    "s_xor_b32 s50, s50, 0x2000\n"
    "s_add_u32 s50, s50, 2097152\n"
    "s_addc_u32 s51, s51, 0\n"
    "s_branch LloopB0_%=\n"
    "LlastB0_%=:\n"
    "v_smfmac_f32_16x16x64_bf16 v[176:179], v[144:147], v[88:95], %[idx]\n"
    "v_smfmac_f32_16x16x64_bf16 v[176:179], v[148:151], v[96:103], %[idx]\n"
    "v_smfmac_f32_16x16x64_bf16 v[176:179], v[152:155], v[104:111], %[idx]\n"
    "v_smfmac_f32_16x16x64_bf16 v[176:179], v[156:159], v[112:119], %[idx]\n"
    "v_smfmac_f32_16x16x64_bf16 v[176:179], v[160:163], v[120:127], %[idx]\n"
    "v_smfmac_f32_16x16x64_bf16 v[176:179], v[164:167], v[128:135], %[idx]\n"
    "v_smfmac_f32_16x16x64_bf16 v[176:179], v[168:171], v[136:143], %[idx]\n"
    "s_nop 7\n"
    "v_permlane32_swap_b32 v176, v178\n"
    "v_permlane32_swap_b32 v177, v179\n"
    "v_add_f32 v176, v176, v178\n"
    "v_add_f32 v177, v177, v179\n"
    "v_cvt_pk_bf16_f32 v245, v176, v177\n"
    "ds_write_b32 v182, v245 offset:12544\n"
    "v_mov_b32 v176, 0\n"
    "v_mov_b32 v177, 0\n"
    "v_mov_b32 v178, 0\n"
    "v_mov_b32 v179, 0\n"
    "s_waitcnt lgkmcnt(0)\n"
    "s_barrier\n"
    "s_waitcnt vmcnt(0)\n"
    "s_barrier\n"
    "s_branch Lend_%=\n"
    "Lfl0B0_%=:\n"
    "s_cmp_eq_u32 s58, 0\n"
    "s_cbranch_scc1 Lflgo0B0_%=\n"
    "s_sub_u32 s58, s58, 1\n"
    "s_sleep 2\n"
    "global_load_dword v180, %[flag], %[base] sc1\n"
    "s_waitcnt vmcnt(0)\n"
    "v_cmp_gt_u32 vcc, s57, v180\n"
    "s_cbranch_vccnz Lfl0B0_%=\n"
    "s_branch Lflgo0B0_%=\n"
    "LrbdB0_%=:\n"
    "s_barrier\n"
    "s_branch LokB0_%=\n"
    "LxsretryB0_%=:\n"
    "s_cmp_eq_u32 s58, 0\n"
    "s_cbranch_scc1 LxsgoB0_%=\n"
    "s_sub_u32 s58, s58, 1\n"
    "global_load_dwordx4 v[208:211], v29, s[44:45] sc1\n"
    "global_load_dwordx4 v[212:215], v29, s[44:45] offset:1024 sc1\n"
    "s_waitcnt vmcnt(0)\n"
    "s_branch LxsB0_%=\n"
    "LsmretryB0_%=:\n"
    "s_cmp_eq_u32 s58, 0\n"
    "s_cbranch_scc1 LsmgoB0_%=\n"
    "s_sub_u32 s58, s58, 1\n"
    "global_load_dwordx4 v[216:219], v29, s[46:47] sc1\n"
    "global_load_dwordx4 v[220:223], v29, s[46:47] offset:1024 sc1\n"
    "s_waitcnt vmcnt(0)\n"
    "s_branch LsmB0_%=\n"
    "LslowB0_%=:\n"
    "s_cmp_lg_u32 s66, 0\n"
    "s_cbranch_scc1 LokB0_%=\n"
    "LnumB0_%=:\n"
    "global_load_dwordx4 v[208:211], %[ra], s[48:49] sc1\n"
    "global_load_dwordx4 v[212:215], %[ra], s[48:49] offset:1024 sc1\n"
    "s_waitcnt vmcnt(0)\n"
    "v_max3_u32 v184, v208, v209, v210\n"
    "v_max3_u32 v184, v184, v211, v212\n"
    "v_max3_u32 v184, v184, v213, v214\n"
    "v_max_u32 v184, v184, v215\n"
    "v_cmp_eq_u32 vcc, -1, v184\n"
    "s_cbranch_vccz LnumgoB0_%=\n"
    "s_cmp_eq_u32 s58, 0\n"
    "s_cbranch_scc1 LnumgoB0_%=\n"
    "s_sub_u32 s58, s58, 1\n"
    "s_branch LnumB0_%=\n"
    "LnumgoB0_%=:\n"
    "v_lshlrev_b32 v216, 16, v208\n"
    "v_and_b32 v217, s68, v208\n"
    "v_lshlrev_b32 v218, 16, v209\n"
    "v_and_b32 v219, s68, v209\n"
    "v_lshlrev_b32 v220, 16, v210\n"
    "v_and_b32 v221, s68, v210\n"
    "v_lshlrev_b32 v222, 16, v211\n"
    "v_and_b32 v223, s68, v211\n"
    "v_max3_f32 v184, v164, v165, s69\n"
    "v_max3_f32 v184, v184, v216, v217\n"
    "v_max3_f32 v184, v184, v218, v219\n"
    "v_max3_f32 v184, v184, v220, v221\n"
    "v_max3_f32 v184, v184, v222, v223\n"
    "v_mov_b32 v185, v184\n"
    "s_nop 1\n"
    "v_permlane16_swap_b32 v184, v185\n"
    "v_max_f32 v184, v184, v185\n"
    "v_mov_b32 v185, v184\n"
    "s_nop 1\n"
    "v_permlane32_swap_b32 v184, v185\n"
    "v_max_f32 v184, v184, v185\n"
    "v_sub_f32 v186, v164, v184\n"
    "v_sub_f32 v187, v165, v184\n"
    "v_sub_f32 v216, v216, v184\n"
    "v_sub_f32 v217, v217, v184\n"
    "v_sub_f32 v218, v218, v184\n"
    "v_sub_f32 v219, v219, v184\n"
    "v_sub_f32 v220, v220, v184\n"
    "v_sub_f32 v221, v221, v184\n"
    "v_sub_f32 v222, v222, v184\n"
    "v_sub_f32 v223, v223, v184\n"
    "v_exp_f32 v216, v216\n"
    "v_exp_f32 v217, v217\n"
    "v_exp_f32 v218, v218\n"
    "v_exp_f32 v219, v219\n"
    "v_exp_f32 v220, v220\n"
    "v_exp_f32 v221, v221\n"
    "v_exp_f32 v222, v222\n"
    "v_exp_f32 v223, v223\n"
    "v_exp_f32 v186, v186\n"
    "v_exp_f32 v187, v187\n"
    "v_cvt_pk_bf16_f32 v208, v216, v217\n"
    "v_cvt_pk_bf16_f32 v209, v218, v219\n"
    "v_cvt_pk_bf16_f32 v210, v220, v221\n"
    "v_cvt_pk_bf16_f32 v211, v222, v223\n"
    "v_cvt_pk_bf16_f32 v246, v186, v187\n"
    "v_lshlrev_b32 v216, 16, v212\n"
    "v_and_b32 v217, s68, v212\n"
    "v_lshlrev_b32 v218, 16, v213\n"
    "v_and_b32 v219, s68, v213\n"
    "v_lshlrev_b32 v220, 16, v214\n"
    "v_and_b32 v221, s68, v214\n"
    "v_lshlrev_b32 v222, 16, v215\n"
    "v_and_b32 v223, s68, v215\n"
    "v_max3_f32 v184, v166, v167, s69\n"
    "v_max3_f32 v184, v184, v216, v217\n"
    "v_max3_f32 v184, v184, v218, v219\n"
    "v_max3_f32 v184, v184, v220, v221\n"
    "v_max3_f32 v184, v184, v222, v223\n"
    "v_mov_b32 v185, v184\n"
    "s_nop 1\n"
    "v_permlane16_swap_b32 v184, v185\n"
    "v_max_f32 v184, v184, v185\n"
    "v_mov_b32 v185, v184\n"
    "s_nop 1\n"
    "v_permlane32_swap_b32 v184, v185\n"
    "v_max_f32 v184, v184, v185\n"
    "v_sub_f32 v186, v166, v184\n"
    "v_sub_f32 v187, v167, v184\n"
    "v_sub_f32 v216, v216, v184\n"
    "v_sub_f32 v217, v217, v184\n"
    "v_sub_f32 v218, v218, v184\n"
    "v_sub_f32 v219, v219, v184\n"
    "v_sub_f32 v220, v220, v184\n"
    "v_sub_f32 v221, v221, v184\n"
    "v_sub_f32 v222, v222, v184\n"
    "v_sub_f32 v223, v223, v184\n"
    "v_exp_f32 v216, v216\n"
    "v_exp_f32 v217, v217\n"
    "v_exp_f32 v218, v218\n"
    "v_exp_f32 v219, v219\n"
    "v_exp_f32 v220, v220\n"
    "v_exp_f32 v221, v221\n"
    "v_exp_f32 v222, v222\n"
    "v_exp_f32 v223, v223\n"
    "v_exp_f32 v186, v186\n"
    "v_exp_f32 v187, v187\n"
    "v_cvt_pk_bf16_f32 v212, v216, v217\n"
    "v_cvt_pk_bf16_f32 v213, v218, v219\n"
    "v_cvt_pk_bf16_f32 v214, v220, v221\n"
    "v_cvt_pk_bf16_f32 v215, v222, v223\n"
    "v_cvt_pk_bf16_f32 v247, v186, v187\n"
    "s_nop 1\n"
    "v_mfma_f32_16x16x32_bf16 v[248:251], v[232:235], v[208:211], 0\n"
    "v_mfma_f32_16x16x32_bf16 v[248:251], v[236:239], v[212:215], v[248:251]\n"
    "v_mfma_f32_16x16x32_bf16 v[144:147], v[200:203], v[208:211], 0\n"
    "v_mfma_f32_16x16x32_bf16 v[148:151], v[204:207], v[212:215], 0\n"
    "s_mov_b32 s66, 1\n"
    "s_nop 7\n"
    "s_branch LtailB0_%=\n"
    "LA0_%=:\n"
    "v_mov_b32 v188, %[rb]\n"
    "v_add_u32 v189, 14336, %[rb]\n"
    "s_lshl_b64 s[78:79], %[epoch], 8\n"
    "Lw4A0_%=:\n"
    "global_load_dwordx2 v[184:185], %[wfl], %[base] offset:-512 sc1\n"
    "s_waitcnt vmcnt(0)\n"
    "Lwc4A0_%=:\n"
    "v_xor_b32 v184, s78, v184\n"
    "v_xor_b32 v185, s79, v185\n"
    "v_subrev_u32 v184, 3, v184\n"
    "v_lshrrev_b32 v184, 3, v184\n"
    "v_or_b32 v184, v184, v185\n"
    "v_cmp_ne_u32 vcc, 0, v184\n"
    "s_cbranch_vccz Lwk4A0_%=\n"
    "s_cmp_eq_u32 s58, 0\n"
    "s_cbranch_scc1 Lwk4A0_%=\n"
    "s_sub_u32 s58, s58, 1\n"
    "s_sleep 6\n"
    "s_branch Lw4A0_%=\n"
    "Lwk4A0_%=:\n"
    "v_add_u32 v186, 4096, v188\n"
    "global_load_dwordx4 v[48:51], v186, %[base] offset:0 sc1\n"
    "global_load_dwordx4 v[52:55], v186, %[base] offset:1024 sc1\n"
    "global_load_dwordx4 v[56:59], v186, %[base] offset:2048 sc1\n"
    "v_add_u32 v186, 4096, v189\n"
    "global_load_dwordx4 v[104:107], v186, %[base] offset:0 sc1\n"
    "global_load_dwordx4 v[108:111], v186, %[base] offset:1024 sc1\n"
    "global_load_dwordx4 v[112:115], v186, %[base] offset:2048 sc1\n"
    "s_waitcnt vmcnt(2)\n"
    "global_load_dwordx2 v[184:185], %[wfl], %[base] offset:0 sc1\n"
    "v_add_u32 v186, 11264, v188\n"
    "global_load_dwordx4 v[76:79], v186, %[base] offset:0 sc1\n"
    "global_load_dwordx4 v[80:83], v186, %[base] offset:1024 sc1\n"
    "global_load_dwordx4 v[84:87], v186, %[base] offset:2048 sc1\n"
    "v_add_u32 v186, 11264, v189\n"
    "global_load_dwordx4 v[132:135], v186, %[base] offset:0 sc1\n"
    "global_load_dwordx4 v[136:139], v186, %[base] offset:1024 sc1\n"
    "global_load_dwordx4 v[140:143], v186, %[base] offset:2048 sc1\n"
    "s_waitcnt vmcnt(6)\n"
    "s_branch Lwc0A0_%=\n"
    "Lw0A0_%=:\n"
    "global_load_dwordx2 v[184:185], %[wfl], %[base] offset:0 sc1\n"
    "s_waitcnt vmcnt(0)\n"
    "Lwc0A0_%=:\n"
    "v_xor_b32 v184, s78, v184\n"
    "v_xor_b32 v185, s79, v185\n"
    "v_subrev_u32 v184, 4, v184\n"
    "v_lshrrev_b32 v184, 3, v184\n"
    "v_or_b32 v184, v184, v185\n"
    "v_cmp_ne_u32 vcc, 0, v184\n"
    "s_cbranch_vccz Lwk0A0_%=\n"
    "s_cmp_eq_u32 s58, 0\n"
    "s_cbranch_scc1 Lwk0A0_%=\n"
    "s_sub_u32 s58, s58, 1\n"
    "s_sleep 6\n"
    "s_branch Lw0A0_%=\n"
    "Lwk0A0_%=:\n"
    "v_add_u32 v186, 0, v188\n"
    "global_load_dwordx4 v[32:35], v186, %[base] offset:0 sc1\n"
    "global_load_dwordx4 v[36:39], v186, %[base] offset:1024 sc1\n"
    "global_load_dwordx4 v[40:43], v186, %[base] offset:2048 sc1\n"
    "global_load_dwordx4 v[44:47], v186, %[base] offset:3072 sc1\n"
    "v_add_u32 v186, 0, v189\n"
    "global_load_dwordx4 v[88:91], v186, %[base] offset:0 sc1\n"
    "global_load_dwordx4 v[92:95], v186, %[base] offset:1024 sc1\n"
    "global_load_dwordx4 v[96:99], v186, %[base] offset:2048 sc1\n"
    "global_load_dwordx4 v[100:103], v186, %[base] offset:3072 sc1\n"
    "v_add_u32 v186, 7168, v188\n"
    "global_load_dwordx4 v[60:63], v186, %[base] offset:0 sc1\n"
    "global_load_dwordx4 v[64:67], v186, %[base] offset:1024 sc1\n"
    "global_load_dwordx4 v[68:71], v186, %[base] offset:2048 sc1\n"
    "global_load_dwordx4 v[72:75], v186, %[base] offset:3072 sc1\n"
    "v_add_u32 v186, 7168, v189\n"
    "global_load_dwordx4 v[116:119], v186, %[base] offset:0 sc1\n"
    "global_load_dwordx4 v[120:123], v186, %[base] offset:1024 sc1\n"
    "global_load_dwordx4 v[124:127], v186, %[base] offset:2048 sc1\n"
    "global_load_dwordx4 v[128:131], v186, %[base] offset:3072 sc1\n"
    "s_setprio 3\n"
    "ds_write_b128 %[lane16], v[172:175] offset:7168\n"
    "ds_write_b128 %[lane16], v[172:175] offset:8192\n"
    "ds_write_b128 %[lane16], v[172:175] offset:9216\n"
    "s_bfe_u32 s59, %[d0], 524296\n"
    "s_lshl_b32 s59, s59, 2\n"
    "s_getpc_b64 s[82:83]\n"
    "Lpca0_%=:\n"
    "s_add_u32 s82, s82, Lv2_%=-Lpca0_%=\n"
    "s_addc_u32 s83, s83, 0\n"
    "s_sub_u32 s82, s82, s59\n"
    "s_subb_u32 s83, s83, 0\n"
    "v_lshlrev_b32 v28, 3, %[lane16]\n"
    "v_or_b32 v28, %[cpy], v28\n"
    "s_lshr_b32 s59, %[cpy], 11\n"
    "s_sub_u32 s59, 4096, s59\n"
    "v_and_b32 v27, 16, %[lane16]\n"
    "v_lshlrev_b32 v27, 8, v27\n"
    "v_and_b32 v26, 0x1e0, %[lane16]\n"
    "v_lshl_add_u32 v27, v26, 2, v27\n"
    "v_add_u32 v27, s59, v27\n"
    "v_mov_b32 v26, 0\n"
    "v_mov_b32 v186, %[go]\n"
    "LgoA0_%=:\n"
    "global_load_dwordx2 v[184:185], v186, %[base] sc1\n"
    "s_waitcnt vmcnt(0)\n"
    "v_readfirstlane_b32 s78, v184\n"
    "v_readfirstlane_b32 s79, v185\n"
    "s_nop 1\n"
    "s_lshr_b64 s[80:81], s[78:79], 8\n"
    "s_cmp_eq_u64 s[80:81], %[epoch]\n"
    "s_cbranch_scc1 LgokA0_%=\n"
    "s_cmp_eq_u32 s58, 0\n"
    "s_cbranch_scc1 LgokA0_%=\n"
    "s_sub_u32 s58, s58, 1\n"
    "s_sleep 2\n"
    "s_branch LgoA0_%=\n"
    "LgokA0_%=:\n"
    "s_bfe_u32 s79, s78, 0x20002\n"
    "s_and_b32 s78, s78, 3\n"
    "s_lshl_b32 s78, s78, 13\n"
    "s_lshl_b32 s79, s79, 13\n"
    "s_xor_b32 s42, s42, s78\n"
    "s_xor_b32 s44, s44, s78\n"
    "s_xor_b32 s46, s46, s79\n"
    "s_xor_b32 s48, s48, s79\n"
    "s_xor_b32 s38, s38, s79\n"
    "LloopA0_%=:\n"
    "s_barrier\n"
    "global_load_dwordx4 v[144:147], v30, s[40:41] sc1\n"
    "s_waitcnt vmcnt(0)\n"
    "LmemA0_%=:\n"
    "ds_write_b128 %[voff16], v[144:147] offset:0\n"
    "v_smfmac_f32_16x16x64_bf16 v[172:175], v[144:147], v[32:39], %[idx]\n"
    "v_max3_u32 v184, v144, v145, v146\n"
    "v_max_u32 v184, v184, v147\n"
    "v_cmp_eq_u32 vcc, -1, v184\n"
    "v_smfmac_f32_16x16x64_bf16 v[176:179], v[144:147], v[88:95], %[idx]\n"
    "s_cbranch_vccnz LmemretryA0_%=\n"
    "LmemgoA0_%=:\n"
    "v_add_u32 v30, %[cpy], %[voff16]\n"
    "s_waitcnt lgkmcnt(0)\n"
    "s_barrier\n"
    "ds_read_b128 v[148:151], %[lane16] offset:1024\n"
    "ds_read_b128 v[152:155], %[lane16] offset:2048\n"
    "ds_read_b128 v[156:159], %[lane16] offset:3072\n"
    "s_barrier\n"
    "ds_read_b128 v[160:163], %[lane16] offset:4096\n"
    "ds_read_b128 v[164:167], %[lane16] offset:5120\n"
    "ds_read_b128 v[168:171], %[lane16] offset:6144\n"
    "ds_read_b128 v[200:203], %[flag] offset:0\n"
    "ds_read_b128 v[204:207], %[flag] offset:1024\n"
    "s_waitcnt lgkmcnt(7)\n"
    "v_smfmac_f32_16x16x64_bf16 v[172:175], v[148:151], v[40:47], %[idx]\n"
    "v_smfmac_f32_16x16x64_bf16 v[176:179], v[148:151], v[96:103], %[idx]\n"
    "s_waitcnt lgkmcnt(6)\n"
    "v_smfmac_f32_16x16x64_bf16 v[172:175], v[152:155], v[48:55], %[idx]\n"
    "v_smfmac_f32_16x16x64_bf16 v[176:179], v[152:155], v[104:111], %[idx]\n"
    "s_waitcnt lgkmcnt(5)\n"
    "v_smfmac_f32_16x16x64_bf16 v[172:175], v[156:159], v[56:63], %[idx]\n"
    "v_smfmac_f32_16x16x64_bf16 v[176:179], v[156:159], v[112:119], %[idx]\n"
    "s_waitcnt lgkmcnt(4)\n"
    "v_smfmac_f32_16x16x64_bf16 v[172:175], v[160:163], v[64:71], %[idx]\n"
    "s_waitcnt lgkmcnt(3)\n"
    "v_smfmac_f32_16x16x64_bf16 v[172:175], v[164:167], v[72:79], %[idx]\n"
    "s_waitcnt lgkmcnt(2)\n"
    "v_smfmac_f32_16x16x64_bf16 v[172:175], v[168:171], v[80:87], %[idx]\n"
    "v_smfmac_f32_16x16x64_bf16 v[176:179], v[160:163], v[120:127], %[idx]\n"
    "s_nop 2\n"
    "v_smfmac_f32_16x16x64_bf16 v[176:179], v[164:167], v[128:135], %[idx]\n"
    "s_nop 2\n"
    "v_smfmac_f32_16x16x64_bf16 v[176:179], v[168:171], v[136:143], %[idx]\n"
    "v_permlane32_swap_b32 v172, v174\n"
    "v_permlane32_swap_b32 v173, v175\n"
    "v_add_f32 v172, v172, v174\n"
    "v_add_f32 v173, v173, v175\n"
    "v_cvt_pk_bf16_f32 v184, v172, v173\n"
    "ds_write_b32 %[ra], v184 offset:7424\n"
    "s_cmp_eq_u32 s56, 63\n"
    "s_nop 0\n"
    "v_permlane32_swap_b32 v176, v178\n"
    "v_permlane32_swap_b32 v177, v179\n"
    "v_add_f32 v176, v176, v178\n"
    "v_add_f32 v177, v177, v179\n"
    "v_cvt_pk_bf16_f32 v185, v176, v177\n"
    "ds_write_b32 %[ra], v185 offset:7744\n"
    "v_mov_b32 v172, 0\n"
    "v_mov_b32 v173, 0\n"
    "v_mov_b32 v174, 0\n"
    "v_mov_b32 v175, 0\n"
    "v_mov_b32 v176, 0\n"
    "v_mov_b32 v177, 0\n"
    "v_mov_b32 v178, 0\n"
    "v_mov_b32 v179, 0\n"
    "s_waitcnt lgkmcnt(0)\n"
    "s_barrier\n"
    "ds_read_b64_tr_b16 v[208:209], %[rc] offset:7424\n"
    "ds_read_b64_tr_b16 v[210:211], %[rc] offset:7488\n"
    "ds_read_b64_tr_b16 v[212:213], %[rc] offset:8704\n"
    "ds_read_b64_tr_b16 v[214:215], %[rc] offset:8768\n"
    "s_cbranch_scc1 LlastA_%=\n"
    "s_waitcnt lgkmcnt(2)\n"
    "v_mfma_f32_16x16x32_bf16 v[188:191], v[208:211], v[200:203], 0\n"
    "s_waitcnt lgkmcnt(0)\n"
    "v_mfma_f32_16x16x32_bf16 v[188:191], v[212:215], v[204:207], v[188:191]\n"
    "s_nop 7\n"
    "v_permlane32_swap_b32 v188, v190\n"
    "v_permlane32_swap_b32 v189, v191\n"
    "s_nop 0\n"
    "v_add_f32_dpp v184, v190, v188 row_ror:8 row_mask:0xf bank_mask:0xf\n"
    "v_add_f32_dpp v185, v191, v189 row_ror:8 row_mask:0xf bank_mask:0xf\n"
    "v_cvt_pk_bf16_f32 v184, v184, v185\n"
    "s_mov_b64 exec, s[60:61]\n"
    "v_min_u32 v184, -2, v184\n"
    "global_store_dword %[comb4], v184, s[46:47] sc1\n"
    "global_store_dword v183, v184, s[46:47] sc1\n"
    "s_mov_b64 exec, -1\n"
    "s_cmp_lg_u32 %[rep], 0\n"
    "s_cbranch_scc1 Lrb1_%=\n"
    "s_setpc_b64 s[82:83]\n"
    ".rept 80\n"
    "s_nop 1\n"
    ".endr\n"
    "Lv2_%=:\n"
    "s_barrier\n"
    "Lrbe1_%=:\n"
    "s_add_u32 s56, s56, 1\n"
    "s_mov_b64 s[40:41], s[42:43]\n"
    "s_and_b32 s59, s56, 15\n"
    "s_cselect_b32 s59, 0, 0x4000\n"
    "s_add_u32 s46, s46, 0x8000\n"
    "s_xor_b32 s46, s46, s59\n"
    "s_add_u32 s44, s44, 0x8000\n"
    "s_xor_b32 s44, s44, s59\n"
    "s_add_u32 s38, s38, 0x8000\n"
    "s_xor_b32 s38, s38, s59\n"
    "s_add_u32 s59, s56, 1\n"
    "s_and_b32 s59, s59, 15\n"
    "s_cselect_b32 s59, 0, 0x4000\n"
    "s_add_u32 s42, s42, 0x8000\n"
    "s_xor_b32 s42, s42, s59\n"
    "s_barrier\n"
    "s_branch LloopA0_%=\n"
    "LmemretryA0_%=:\n"
    "s_cmp_eq_u32 s58, 0\n"
    "s_cbranch_scc1 LmemgoA0_%=\n"
    "s_sub_u32 s58, s58, 1\n"
    "s_cmp_eq_u32 %[rep], 0\n"
    "s_cbranch_scc1 LrsA0_%=\n"
    "s_sleep 4\n"
    "LrsA0_%=:\n"
    "global_load_dwordx4 v[144:147], v30, s[40:41] sc1\n"
    "s_nop 7\n"
    "v_mov_b32 v172, 0\n"
    "v_mov_b32 v173, 0\n"
    "v_mov_b32 v174, 0\n"
    "v_mov_b32 v175, 0\n"
    "v_mov_b32 v176, 0\n"
    "v_mov_b32 v177, 0\n"
    "v_mov_b32 v178, 0\n"
    "v_mov_b32 v179, 0\n"
    "s_waitcnt vmcnt(0)\n"
    "s_branch LmemA0_%=\n"
    "Lrb1_%=:\n"
    "s_nop 9\n"
    "global_load_dword v187, v28, s[44:45] sc1\n"
    "s_mov_b64 s[48:49], s[38:39]\n"
    "s_xor_b32 s48, s48, 0x4000\n"
    "s_barrier\n"
    "Lsg1_%=:\n"
    "global_load_dword v186, v28, s[46:47] sc1\n"
    "s_waitcnt vmcnt(0)\n"
    "v_max_u32 v186, v186, v187\n"
    "v_cmp_eq_u32 vcc, -1, v186\n"
    "s_cbranch_vccnz Lsgr1_%=\n"
    "Lsgo1_%=:\n"
    "s_mov_b32 exec_hi, 0\n"
    "global_store_dword v27, v26, s[38:39] offset:-4096 sc1\n"
    "s_mov_b32 exec_lo, 0x55555555\n"
    "global_store_dword v27, v26, s[48:49] offset:-4096 sc1\n"
    "s_mov_b64 exec, -1\n"
    "s_branch Lrbe1_%=\n"
    "Lsgr1_%=:\n"
    "s_cmp_eq_u32 s58, 0\n"
    "s_cbranch_scc1 Lsgo1_%=\n"
    "s_sub_u32 s58, s58, 1\n"
    "s_sleep 4\n"
    "global_load_dword v187, v28, s[44:45] sc1\n"
    "s_branch Lsg1_%=\n"
    "LA1_%=:\n"
    "v_mov_b32 v188, %[rb]\n"
    "v_add_u32 v189, 14336, %[rb]\n"
    "s_lshl_b64 s[78:79], %[epoch], 8\n"
    "Lw4A1_%=:\n"
    "global_load_dwordx2 v[184:185], %[wfl], %[base] offset:-512 sc1\n"
    "s_waitcnt vmcnt(0)\n"
    "Lwc4A1_%=:\n"
    "v_xor_b32 v184, s78, v184\n"
    "v_xor_b32 v185, s79, v185\n"
    "v_subrev_u32 v184, 3, v184\n"
    "v_lshrrev_b32 v184, 3, v184\n"
    "v_or_b32 v184, v184, v185\n"
    "v_cmp_ne_u32 vcc, 0, v184\n"
    "s_cbranch_vccz Lwk4A1_%=\n"
    "s_cmp_eq_u32 s58, 0\n"
    "s_cbranch_scc1 Lwk4A1_%=\n"
    "s_sub_u32 s58, s58, 1\n"
    "s_sleep 6\n"
    "s_branch Lw4A1_%=\n"
    "Lwk4A1_%=:\n"
    "v_add_u32 v186, 4096, v188\n"
    "global_load_dwordx4 v[48:51], v186, %[base] offset:0 sc1\n"
    "global_load_dwordx4 v[52:55], v186, %[base] offset:1024 sc1\n"
    "global_load_dwordx4 v[56:59], v186, %[base] offset:2048 sc1\n"
    "v_add_u32 v186, 4096, v189\n"
    "global_load_dwordx4 v[104:107], v186, %[base] offset:0 sc1\n"
    "global_load_dwordx4 v[108:111], v186, %[base] offset:1024 sc1\n"
    "global_load_dwordx4 v[112:115], v186, %[base] offset:2048 sc1\n"
    "s_waitcnt vmcnt(2)\n"
    "global_load_dwordx2 v[184:185], %[wfl], %[base] offset:0 sc1\n"
    "v_add_u32 v186, 11264, v188\n"
    "global_load_dwordx4 v[76:79], v186, %[base] offset:0 sc1\n"
    "global_load_dwordx4 v[80:83], v186, %[base] offset:1024 sc1\n"
    "global_load_dwordx4 v[84:87], v186, %[base] offset:2048 sc1\n"
    "v_add_u32 v186, 11264, v189\n"
    "global_load_dwordx4 v[132:135], v186, %[base] offset:0 sc1\n"
    "global_load_dwordx4 v[136:139], v186, %[base] offset:1024 sc1\n"
    "global_load_dwordx4 v[140:143], v186, %[base] offset:2048 sc1\n"
    "s_waitcnt vmcnt(6)\n"
    "s_branch Lwc0A1_%=\n"
    "Lw0A1_%=:\n"
    "global_load_dwordx2 v[184:185], %[wfl], %[base] offset:0 sc1\n"
    "s_waitcnt vmcnt(0)\n"
    "Lwc0A1_%=:\n"
    "v_xor_b32 v184, s78, v184\n"
    "v_xor_b32 v185, s79, v185\n"
    "v_subrev_u32 v184, 4, v184\n"
    "v_lshrrev_b32 v184, 3, v184\n"
    "v_or_b32 v184, v184, v185\n"
    "v_cmp_ne_u32 vcc, 0, v184\n"
    "s_cbranch_vccz Lwk0A1_%=\n"
    "s_cmp_eq_u32 s58, 0\n"
    "s_cbranch_scc1 Lwk0A1_%=\n"
    "s_sub_u32 s58, s58, 1\n"
    "s_sleep 6\n"
    "s_branch Lw0A1_%=\n"
    "Lwk0A1_%=:\n"
    "v_add_u32 v186, 0, v188\n"
    "global_load_dwordx4 v[32:35], v186, %[base] offset:0 sc1\n"
    "global_load_dwordx4 v[36:39], v186, %[base] offset:1024 sc1\n"
    "global_load_dwordx4 v[40:43], v186, %[base] offset:2048 sc1\n"
    "global_load_dwordx4 v[44:47], v186, %[base] offset:3072 sc1\n"
    "v_add_u32 v186, 0, v189\n"
    "global_load_dwordx4 v[88:91], v186, %[base] offset:0 sc1\n"
    "global_load_dwordx4 v[92:95], v186, %[base] offset:1024 sc1\n"
    "global_load_dwordx4 v[96:99], v186, %[base] offset:2048 sc1\n"
    "global_load_dwordx4 v[100:103], v186, %[base] offset:3072 sc1\n"
    "v_add_u32 v186, 7168, v188\n"
    "global_load_dwordx4 v[60:63], v186, %[base] offset:0 sc1\n"
    "global_load_dwordx4 v[64:67], v186, %[base] offset:1024 sc1\n"
    "global_load_dwordx4 v[68:71], v186, %[base] offset:2048 sc1\n"
    "global_load_dwordx4 v[72:75], v186, %[base] offset:3072 sc1\n"
    "v_add_u32 v186, 7168, v189\n"
    "global_load_dwordx4 v[116:119], v186, %[base] offset:0 sc1\n"
    "global_load_dwordx4 v[120:123], v186, %[base] offset:1024 sc1\n"
    "global_load_dwordx4 v[124:127], v186, %[base] offset:2048 sc1\n"
    "global_load_dwordx4 v[128:131], v186, %[base] offset:3072 sc1\n"
    "s_setprio 3\n"
    "s_bfe_u32 s59, %[d0], 524304\n"
    "s_lshl_b32 s59, s59, 2\n"
    "s_getpc_b64 s[82:83]\n"
    "Lpca1_%=:\n"
    "s_add_u32 s82, s82, Lc1_%=-Lpca1_%=\n"
    "s_addc_u32 s83, s83, 0\n"
    "s_sub_u32 s82, s82, s59\n"
    "s_subb_u32 s83, s83, 0\n"
    "s_bfe_u32 s59, %[d1], 524288\n"
    "s_lshl_b32 s59, s59, 2\n"
    "s_getpc_b64 s[54:55]\n"
    "Lpca2_%=:\n"
    "s_add_u32 s54, s54, Lp1_%=-Lpca2_%=\n"
    "s_addc_u32 s55, s55, 0\n"
    "s_sub_u32 s54, s54, s59\n"
    "s_subb_u32 s55, s55, 0\n"
    "s_waitcnt vmcnt(0)\n"
    "v_mov_b32 v186, %[go]\n"
    "LgoA1_%=:\n"
    "global_load_dwordx2 v[184:185], v186, %[base] sc1\n"
    "s_waitcnt vmcnt(0)\n"
    "v_readfirstlane_b32 s78, v184\n"
    "v_readfirstlane_b32 s79, v185\n"
    "s_nop 1\n"
    "s_lshr_b64 s[80:81], s[78:79], 8\n"
    "s_cmp_eq_u64 s[80:81], %[epoch]\n"
    "s_cbranch_scc1 LgokA1_%=\n"
    "s_cmp_eq_u32 s58, 0\n"
    "s_cbranch_scc1 LgokA1_%=\n"
    "s_sub_u32 s58, s58, 1\n"
    "s_sleep 2\n"
    "s_branch LgoA1_%=\n"
    "LgokA1_%=:\n"
    "s_bfe_u32 s79, s78, 0x20002\n"
    "s_and_b32 s78, s78, 3\n"
    "s_lshl_b32 s78, s78, 13\n"
    "s_lshl_b32 s79, s79, 13\n"
    "s_xor_b32 s42, s42, s78\n"
    "s_xor_b32 s44, s44, s78\n"
    "s_xor_b32 s46, s46, s79\n"
    "s_xor_b32 s48, s48, s79\n"
    "s_xor_b32 s38, s38, s79\n"
    "LloopA1_%=:\n"
    "s_barrier\n"
    "global_load_dwordx4 v[148:151], v30, s[40:41] sc1\n"
    "s_waitcnt vmcnt(0)\n"
    "LmemA1_%=:\n"
    "ds_write_b128 %[voff16], v[148:151] offset:0\n"
    "v_smfmac_f32_16x16x64_bf16 v[172:175], v[148:151], v[40:47], %[idx]\n"
    "v_max3_u32 v184, v148, v149, v150\n"
    "v_max_u32 v184, v184, v151\n"
    "v_cmp_eq_u32 vcc, -1, v184\n"
    "v_smfmac_f32_16x16x64_bf16 v[176:179], v[148:151], v[96:103], %[idx]\n"
    "s_cbranch_vccnz LmemretryA1_%=\n"
    "LmemgoA1_%=:\n"
    "v_add_u32 v30, %[cpy], %[voff16]\n"
    "s_waitcnt lgkmcnt(0)\n"
    "s_barrier\n"
    "ds_read_b128 v[152:155], %[lane16] offset:2048\n"
    "ds_read_b128 v[156:159], %[lane16] offset:3072\n"
    "ds_read_b128 v[144:147], %[lane16] offset:0\n"
    "s_barrier\n"
    "ds_read_b128 v[160:163], %[lane16] offset:4096\n"
    "ds_read_b128 v[164:167], %[lane16] offset:5120\n"
    "ds_read_b128 v[168:171], %[lane16] offset:6144\n"
    "s_waitcnt lgkmcnt(5)\n"
    "v_smfmac_f32_16x16x64_bf16 v[172:175], v[152:155], v[48:55], %[idx]\n"
    "v_smfmac_f32_16x16x64_bf16 v[176:179], v[152:155], v[104:111], %[idx]\n"
    "s_waitcnt lgkmcnt(4)\n"
    "v_smfmac_f32_16x16x64_bf16 v[172:175], v[156:159], v[56:63], %[idx]\n"
    "v_smfmac_f32_16x16x64_bf16 v[176:179], v[156:159], v[112:119], %[idx]\n"
    "s_waitcnt lgkmcnt(3)\n"
    "v_smfmac_f32_16x16x64_bf16 v[172:175], v[144:147], v[32:39], %[idx]\n"
    "v_smfmac_f32_16x16x64_bf16 v[176:179], v[144:147], v[88:95], %[idx]\n"
    "s_waitcnt lgkmcnt(2)\n"
    "v_smfmac_f32_16x16x64_bf16 v[172:175], v[160:163], v[64:71], %[idx]\n"
    "s_waitcnt lgkmcnt(1)\n"
    "v_smfmac_f32_16x16x64_bf16 v[172:175], v[164:167], v[72:79], %[idx]\n"
    "s_waitcnt lgkmcnt(0)\n"
    "v_smfmac_f32_16x16x64_bf16 v[172:175], v[168:171], v[80:87], %[idx]\n"
    "v_smfmac_f32_16x16x64_bf16 v[176:179], v[160:163], v[120:127], %[idx]\n"
    "s_nop 2\n"
    "v_smfmac_f32_16x16x64_bf16 v[176:179], v[164:167], v[128:135], %[idx]\n"
    "s_nop 2\n"
    "v_smfmac_f32_16x16x64_bf16 v[176:179], v[168:171], v[136:143], %[idx]\n"
    "v_permlane32_swap_b32 v172, v174\n"
    "v_permlane32_swap_b32 v173, v175\n"
    "v_add_f32 v172, v172, v174\n"
    "v_add_f32 v173, v173, v175\n"
    "v_cvt_pk_bf16_f32 v184, v172, v173\n"
    "ds_write_b32 %[ra], v184 offset:7424\n"
    "s_cmp_eq_u32 s56, 63\n"
    "s_nop 0\n"
    "v_permlane32_swap_b32 v176, v178\n"
    "v_permlane32_swap_b32 v177, v179\n"
    "v_add_f32 v176, v176, v178\n"
    "v_add_f32 v177, v177, v179\n"
    "v_cvt_pk_bf16_f32 v185, v176, v177\n"
    "ds_write_b32 %[ra], v185 offset:7744\n"
    "v_mov_b32 v172, 0\n"
    "v_mov_b32 v173, 0\n"
    "v_mov_b32 v174, 0\n"
    "v_mov_b32 v175, 0\n"
    "v_mov_b32 v176, 0\n"
    "v_mov_b32 v177, 0\n"
    "v_mov_b32 v178, 0\n"
    "v_mov_b32 v179, 0\n"
    "s_waitcnt lgkmcnt(0)\n"
    "s_barrier\n"
    "s_cbranch_scc1 LlastA_%=\n"
    "s_mov_b64 s[46:47], s[38:39]\n"
    "s_add_u32 s56, s56, 1\n"
    "s_mov_b64 s[40:41], s[42:43]\n"
    "s_and_b32 s59, s56, 15\n"
    "s_cselect_b32 s59, 0, 0x4000\n"
    "s_add_u32 s38, s38, 0x8000\n"
    "s_xor_b32 s38, s38, s59\n"
    "s_add_u32 s59, s56, 1\n"
    "s_and_b32 s59, s59, 15\n"
    "s_cselect_b32 s59, 0, 0x4000\n"
    "s_add_u32 s42, s42, 0x8000\n"
    "s_xor_b32 s42, s42, s59\n"
    "s_mov_b64 s[80:81], -1\n"
    "s_barrier\n"
    "s_sleep 4\n"
    "s_setpc_b64 s[54:55]\n"
    ".rept 80\n"
    "s_nop 1\n"
    ".endr\n"
    "Lp1_%=:\n"
    "s_load_dword s80, s[46:47], 0\n"
    "s_load_dword s81, s[46:47], 2048\n"
    "s_nop 9\n"
    "s_load_dword s80, s[46:47], 128\n"
    "s_load_dword s81, s[46:47], 2176\n"
    "s_nop 9\n"
    "s_load_dword s80, s[46:47], 256\n"
    "s_load_dword s81, s[46:47], 2304\n"
    "s_nop 9\n"
    "s_load_dword s80, s[46:47], 384\n"
    "s_load_dword s81, s[46:47], 2432\n"
    "s_nop 9\n"
    "s_load_dword s80, s[46:47], 512\n"
    "s_load_dword s81, s[46:47], 2560\n"
    "s_nop 9\n"
    "s_barrier\n"
    "s_load_dword s80, s[46:47], 640\n"
    "s_load_dword s81, s[46:47], 2688\n"
    "s_cmp_eq_u64 s[80:81], 0\n"
    "s_cbranch_scc1 Lseen1_%=\n"
    "s_nop 1\n"
    "s_cmp_eq_u64 s[80:81], 0\n"
    "s_cbranch_scc1 Lseen1_%=\n"
    "s_nop 3\n"
    "s_load_dword s80, s[46:47], 768\n"
    "s_load_dword s81, s[46:47], 2816\n"
    "s_cmp_eq_u64 s[80:81], 0\n"
    "s_cbranch_scc1 Lseen1_%=\n"
    "s_nop 1\n"
    "s_cmp_eq_u64 s[80:81], 0\n"
    "s_cbranch_scc1 Lseen1_%=\n"
    "s_nop 3\n"
    "s_load_dword s80, s[46:47], 896\n"
    "s_load_dword s81, s[46:47], 2944\n"
    "s_cmp_eq_u64 s[80:81], 0\n"
    "s_cbranch_scc1 Lseen1_%=\n"
    "s_nop 1\n"
    "s_cmp_eq_u64 s[80:81], 0\n"
    "s_cbranch_scc1 Lseen1_%=\n"
    "s_nop 3\n"
    "s_load_dword s80, s[46:47], 1024\n"
    "s_load_dword s81, s[46:47], 3072\n"
    "s_cmp_eq_u64 s[80:81], 0\n"
    "s_cbranch_scc1 Lseen1_%=\n"
    "s_nop 1\n"
    "s_cmp_eq_u64 s[80:81], 0\n"
    "s_cbranch_scc1 Lseen1_%=\n"
    "s_nop 3\n"
    "s_load_dword s80, s[46:47], 1152\n"
    "s_load_dword s81, s[46:47], 3200\n"
    "s_cmp_eq_u64 s[80:81], 0\n"
    "s_cbranch_scc1 Lseen1_%=\n"
    "s_nop 1\n"
    "s_cmp_eq_u64 s[80:81], 0\n"
    "s_cbranch_scc1 Lseen1_%=\n"
    "s_nop 3\n"
    "s_load_dword s80, s[46:47], 1280\n"
    "s_load_dword s81, s[46:47], 3328\n"
    "s_cmp_eq_u64 s[80:81], 0\n"
    "s_cbranch_scc1 Lseen1_%=\n"
    "s_nop 1\n"
    "s_cmp_eq_u64 s[80:81], 0\n"
    "s_cbranch_scc1 Lseen1_%=\n"
    "s_nop 3\n"
    "s_load_dword s80, s[46:47], 1408\n"
    "s_load_dword s81, s[46:47], 3456\n"
    "s_cmp_eq_u64 s[80:81], 0\n"
    "s_cbranch_scc1 Lseen1_%=\n"
    "s_nop 1\n"
    "s_cmp_eq_u64 s[80:81], 0\n"
    "s_cbranch_scc1 Lseen1_%=\n"
    "s_nop 3\n"
    "s_load_dword s80, s[46:47], 1536\n"
    "s_load_dword s81, s[46:47], 3584\n"
    "s_cmp_eq_u64 s[80:81], 0\n"
    "s_cbranch_scc1 Lseen1_%=\n"
    "s_nop 1\n"
    "s_cmp_eq_u64 s[80:81], 0\n"
    "s_cbranch_scc1 Lseen1_%=\n"
    "s_nop 3\n"
    "s_load_dword s80, s[46:47], 1664\n"
    "s_load_dword s81, s[46:47], 3712\n"
    "s_cmp_eq_u64 s[80:81], 0\n"
    "s_cbranch_scc1 Lseen1_%=\n"
    "s_nop 1\n"
    "s_cmp_eq_u64 s[80:81], 0\n"
    "s_cbranch_scc1 Lseen1_%=\n"
    "s_nop 3\n"
    "s_load_dword s80, s[46:47], 1792\n"
    "s_load_dword s81, s[46:47], 3840\n"
    "s_cmp_eq_u64 s[80:81], 0\n"
    "s_cbranch_scc1 Lseen1_%=\n"
    "s_nop 1\n"
    "s_cmp_eq_u64 s[80:81], 0\n"
    "s_cbranch_scc1 Lseen1_%=\n"
    "s_nop 3\n"
    "s_load_dword s80, s[46:47], 1920\n"
    "s_load_dword s81, s[46:47], 3968\n"
    "s_cmp_eq_u64 s[80:81], 0\n"
    "s_cbranch_scc1 Lseen1_%=\n"
    "s_nop 1\n"
    "s_cmp_eq_u64 s[80:81], 0\n"
    "s_cbranch_scc1 Lseen1_%=\n"
    "s_nop 3\n"
    "s_mov_b32 s59, 10\n"
    "Ldq1_%=:\n"
    "s_cmp_eq_u64 s[80:81], 0\n"
    "s_cbranch_scc1 Lseen1_%=\n"
    "s_sub_u32 s59, s59, 1\n"
    "s_cbranch_scc0 Ldq1_%=\n"
    "s_add_u32 s78, %[own], 2048\n"
    "s_mov_b32 s59, 200\n"
    "Ldr1_%=:\n"
    "s_load_dword s80, s[46:47], %[own] glc\n"
    "s_load_dword s81, s[46:47], s78 glc\n"
    "s_waitcnt lgkmcnt(0)\n"
    "s_cmp_eq_u64 s[80:81], 0\n"
    "s_cbranch_scc1 Lseen1_%=\n"
    "s_sub_u32 s59, s59, 1\n"
    "s_cbranch_scc0 Ldr1_%=\n"
    "Lseen1_%=:\n"
    "s_setpc_b64 s[82:83]\n"
    ".rept 80\n"
    "s_nop 1\n"
    ".endr\n"
    "Lc1_%=:\n"
    "s_branch LloopA1_%=\n"
    "LlastA_%=:\n"
    "s_waitcnt lgkmcnt(0)\n"
    "s_barrier\n"
    "s_waitcnt vmcnt(0)\n"
    "s_barrier\n"
    "s_branch Lend_%=\n"
    "LmemretryA1_%=:\n"
    "s_cmp_eq_u32 s58, 0\n"
    "s_cbranch_scc1 LmemgoA1_%=\n"
    "s_sub_u32 s58, s58, 1\n"
    "s_cmp_eq_u32 %[rep], 0\n"
    "s_cbranch_scc1 LrsA1_%=\n"
    "s_sleep 4\n"
    "LrsA1_%=:\n"
    "global_load_dwordx4 v[148:151], v30, s[40:41] sc1\n"
    "s_nop 7\n"
    "v_mov_b32 v172, 0\n"
    "v_mov_b32 v173, 0\n"
    "v_mov_b32 v174, 0\n"
    "v_mov_b32 v175, 0\n"
    "v_mov_b32 v176, 0\n"
    "v_mov_b32 v177, 0\n"
    "v_mov_b32 v178, 0\n"
    "v_mov_b32 v179, 0\n"
    "s_waitcnt vmcnt(0)\n"
    "s_branch LmemA1_%=\n"
    "LA2_%=:\n"
    "v_mov_b32 v188, %[rb]\n"
    "v_add_u32 v189, 14336, %[rb]\n"
    "s_lshl_b64 s[78:79], %[epoch], 8\n"
    "Lw4A2_%=:\n"
    "global_load_dwordx2 v[184:185], %[wfl], %[base] offset:-512 sc1\n"
    "s_waitcnt vmcnt(0)\n"
    "Lwc4A2_%=:\n"
    "v_xor_b32 v184, s78, v184\n"
    "v_xor_b32 v185, s79, v185\n"
    "v_subrev_u32 v184, 3, v184\n"
    "v_lshrrev_b32 v184, 3, v184\n"
    "v_or_b32 v184, v184, v185\n"
    "v_cmp_ne_u32 vcc, 0, v184\n"
    "s_cbranch_vccz Lwk4A2_%=\n"
    "s_cmp_eq_u32 s58, 0\n"
    "s_cbranch_scc1 Lwk4A2_%=\n"
    "s_sub_u32 s58, s58, 1\n"
    "s_sleep 6\n"
    "s_branch Lw4A2_%=\n"
    "Lwk4A2_%=:\n"
    "v_add_u32 v186, 4096, v188\n"
    "global_load_dwordx4 v[48:51], v186, %[base] offset:0 sc1\n"
    "global_load_dwordx4 v[52:55], v186, %[base] offset:1024 sc1\n"
    "global_load_dwordx4 v[56:59], v186, %[base] offset:2048 sc1\n"
    "v_add_u32 v186, 4096, v189\n"
    "global_load_dwordx4 v[104:107], v186, %[base] offset:0 sc1\n"
    "global_load_dwordx4 v[108:111], v186, %[base] offset:1024 sc1\n"
    "global_load_dwordx4 v[112:115], v186, %[base] offset:2048 sc1\n"
    "s_waitcnt vmcnt(2)\n"
    "global_load_dwordx2 v[184:185], %[wfl], %[base] offset:0 sc1\n"
    "v_add_u32 v186, 11264, v188\n"
    "global_load_dwordx4 v[76:79], v186, %[base] offset:0 sc1\n"
    "global_load_dwordx4 v[80:83], v186, %[base] offset:1024 sc1\n"
    "global_load_dwordx4 v[84:87], v186, %[base] offset:2048 sc1\n"
    "v_add_u32 v186, 11264, v189\n"
    "global_load_dwordx4 v[132:135], v186, %[base] offset:0 sc1\n"
    "global_load_dwordx4 v[136:139], v186, %[base] offset:1024 sc1\n"
    "global_load_dwordx4 v[140:143], v186, %[base] offset:2048 sc1\n"
    "s_waitcnt vmcnt(6)\n"
    "s_branch Lwc0A2_%=\n"
    "Lw0A2_%=:\n"
    "global_load_dwordx2 v[184:185], %[wfl], %[base] offset:0 sc1\n"
    "s_waitcnt vmcnt(0)\n"
    "Lwc0A2_%=:\n"
    "v_xor_b32 v184, s78, v184\n"
    "v_xor_b32 v185, s79, v185\n"
    "v_subrev_u32 v184, 4, v184\n"
    "v_lshrrev_b32 v184, 3, v184\n"
    "v_or_b32 v184, v184, v185\n"
    "v_cmp_ne_u32 vcc, 0, v184\n"
    "s_cbranch_vccz Lwk0A2_%=\n"
    "s_cmp_eq_u32 s58, 0\n"
    "s_cbranch_scc1 Lwk0A2_%=\n"
    "s_sub_u32 s58, s58, 1\n"
    "s_sleep 6\n"
    "s_branch Lw0A2_%=\n"
    "Lwk0A2_%=:\n"
    "v_add_u32 v186, 0, v188\n"
    "global_load_dwordx4 v[32:35], v186, %[base] offset:0 sc1\n"
    "global_load_dwordx4 v[36:39], v186, %[base] offset:1024 sc1\n"
    "global_load_dwordx4 v[40:43], v186, %[base] offset:2048 sc1\n"
    "global_load_dwordx4 v[44:47], v186, %[base] offset:3072 sc1\n"
    "v_add_u32 v186, 0, v189\n"
    "global_load_dwordx4 v[88:91], v186, %[base] offset:0 sc1\n"
    "global_load_dwordx4 v[92:95], v186, %[base] offset:1024 sc1\n"
    "global_load_dwordx4 v[96:99], v186, %[base] offset:2048 sc1\n"
    "global_load_dwordx4 v[100:103], v186, %[base] offset:3072 sc1\n"
    "v_add_u32 v186, 7168, v188\n"
    "global_load_dwordx4 v[60:63], v186, %[base] offset:0 sc1\n"
    "global_load_dwordx4 v[64:67], v186, %[base] offset:1024 sc1\n"
    "global_load_dwordx4 v[68:71], v186, %[base] offset:2048 sc1\n"
    "global_load_dwordx4 v[72:75], v186, %[base] offset:3072 sc1\n"
    "v_add_u32 v186, 7168, v189\n"
    "global_load_dwordx4 v[116:119], v186, %[base] offset:0 sc1\n"
    "global_load_dwordx4 v[120:123], v186, %[base] offset:1024 sc1\n"
    "global_load_dwordx4 v[124:127], v186, %[base] offset:2048 sc1\n"
    "global_load_dwordx4 v[128:131], v186, %[base] offset:3072 sc1\n"
    "s_setprio 3\n"
    "s_mov_b64 s[52:53], %[base]\n"
    "s_add_u32 s52, s52, %[oby]\n"
    "s_addc_u32 s53, s53, 0\n"
    "s_mov_b64 s[50:51], %[base]\n"
    "s_add_u32 s50, s50, %[opf]\n"
    "s_addc_u32 s51, s51, 0\n"
    "v_mov_b32 v186, %[go]\n"
    "LgoA2_%=:\n"
    "global_load_dwordx2 v[184:185], v186, %[base] sc1\n"
    "s_waitcnt vmcnt(0)\n"
    "v_readfirstlane_b32 s78, v184\n"
    "v_readfirstlane_b32 s79, v185\n"
    "s_nop 1\n"
    "s_lshr_b64 s[80:81], s[78:79], 8\n"
    "s_cmp_eq_u64 s[80:81], %[epoch]\n"
    "s_cbranch_scc1 LgokA2_%=\n"
    "s_cmp_eq_u32 s58, 0\n"
    "s_cbranch_scc1 LgokA2_%=\n"
    "s_sub_u32 s58, s58, 1\n"
    "s_sleep 2\n"
    "s_branch LgoA2_%=\n"
    "LgokA2_%=:\n"
    "s_bfe_u32 s79, s78, 0x20002\n"
    "s_and_b32 s78, s78, 3\n"
    "s_lshl_b32 s78, s78, 13\n"
    "s_lshl_b32 s79, s79, 13\n"
    "s_xor_b32 s42, s42, s78\n"
    "s_xor_b32 s44, s44, s78\n"
    "s_xor_b32 s46, s46, s79\n"
    "s_xor_b32 s48, s48, s79\n"
    "s_xor_b32 s38, s38, s79\n"
    "s_sub_u32 s67, %[opf], %[oby]\n"
    "s_and_b32 s78, s78, 0x2000\n"
    "s_xor_b32 s52, s52, s78\n"
    "s_sub_u32 s50, s50, 0x80000\n"
    "s_subb_u32 s51, s51, 0\n"
    "LloopA2_%=:\n"
    "s_barrier\n"
    "global_load_dwordx4 v[152:155], v30, s[40:41] sc1\n"
    "s_waitcnt vmcnt(0)\n"
    "LmemA2_%=:\n"
    "ds_write_b128 %[voff16], v[152:155] offset:0\n"
    "v_smfmac_f32_16x16x64_bf16 v[172:175], v[152:155], v[48:55], %[idx]\n"
    "v_max3_u32 v184, v152, v153, v154\n"
    "v_max_u32 v184, v184, v155\n"
    "v_cmp_eq_u32 vcc, -1, v184\n"
    "v_smfmac_f32_16x16x64_bf16 v[176:179], v[152:155], v[104:111], %[idx]\n"
    "s_cbranch_vccnz LmemretryA2_%=\n"
    "LmemgoA2_%=:\n"
    "v_add_u32 v30, %[cpy], %[voff16]\n"
    "s_waitcnt lgkmcnt(0)\n"
    "s_barrier\n"
    "ds_read_b128 v[156:159], %[lane16] offset:3072\n"
    "ds_read_b128 v[144:147], %[lane16] offset:0\n"
    "ds_read_b128 v[148:151], %[lane16] offset:1024\n"
    "s_barrier\n"
    "ds_read_b128 v[160:163], %[lane16] offset:4096\n"
    "ds_read_b128 v[164:167], %[lane16] offset:5120\n"
    "ds_read_b128 v[168:171], %[lane16] offset:6144\n"
    "s_waitcnt lgkmcnt(5)\n"
    "v_smfmac_f32_16x16x64_bf16 v[172:175], v[156:159], v[56:63], %[idx]\n"
    "v_smfmac_f32_16x16x64_bf16 v[176:179], v[156:159], v[112:119], %[idx]\n"
    "s_waitcnt lgkmcnt(4)\n"
    "v_smfmac_f32_16x16x64_bf16 v[172:175], v[144:147], v[32:39], %[idx]\n"
    "v_smfmac_f32_16x16x64_bf16 v[176:179], v[144:147], v[88:95], %[idx]\n"
    "s_waitcnt lgkmcnt(3)\n"
    "v_smfmac_f32_16x16x64_bf16 v[172:175], v[148:151], v[40:47], %[idx]\n"
    "v_smfmac_f32_16x16x64_bf16 v[176:179], v[148:151], v[96:103], %[idx]\n"
    "s_waitcnt lgkmcnt(2)\n"
    "v_smfmac_f32_16x16x64_bf16 v[172:175], v[160:163], v[64:71], %[idx]\n"
    "s_waitcnt lgkmcnt(1)\n"
    "v_smfmac_f32_16x16x64_bf16 v[172:175], v[164:167], v[72:79], %[idx]\n"
    "s_waitcnt lgkmcnt(0)\n"
    "v_smfmac_f32_16x16x64_bf16 v[172:175], v[168:171], v[80:87], %[idx]\n"
    "v_smfmac_f32_16x16x64_bf16 v[176:179], v[160:163], v[120:127], %[idx]\n"
    "s_nop 2\n"
    "v_smfmac_f32_16x16x64_bf16 v[176:179], v[164:167], v[128:135], %[idx]\n"
    "s_nop 2\n"
    "v_smfmac_f32_16x16x64_bf16 v[176:179], v[168:171], v[136:143], %[idx]\n"
    "v_permlane32_swap_b32 v172, v174\n"
    "v_permlane32_swap_b32 v173, v175\n"
    "v_add_f32 v172, v172, v174\n"
    "v_add_f32 v173, v173, v175\n"
    "v_cvt_pk_bf16_f32 v184, v172, v173\n"
    "ds_write_b32 %[ra], v184 offset:7424\n"
    "s_cmp_eq_u32 s56, 63\n"
    "s_nop 0\n"
    "v_permlane32_swap_b32 v176, v178\n"
    "v_permlane32_swap_b32 v177, v179\n"
    "v_add_f32 v176, v176, v178\n"
    "v_add_f32 v177, v177, v179\n"
    "v_cvt_pk_bf16_f32 v185, v176, v177\n"
    "ds_write_b32 %[ra], v185 offset:7744\n"
    "v_mov_b32 v172, 0\n"
    "v_mov_b32 v173, 0\n"
    "v_mov_b32 v174, 0\n"
    "v_mov_b32 v175, 0\n"
    "v_mov_b32 v176, 0\n"
    "v_mov_b32 v177, 0\n"
    "v_mov_b32 v178, 0\n"
    "v_mov_b32 v179, 0\n"
    "s_waitcnt lgkmcnt(0)\n"
    "s_barrier\n"
    "s_nop 15\n"
    "ds_read_b64_tr_b16 v[208:209], %[rc] offset:7424\n"
    "ds_read_b64_tr_b16 v[210:211], %[rc] offset:7488\n"
    "ds_read_b64_tr_b16 v[212:213], %[rc] offset:8704\n"
    "ds_read_b64_tr_b16 v[214:215], %[rc] offset:8768\n"
    "s_waitcnt lgkmcnt(0)\n"
    "s_cbranch_scc1 LlastA2_%=\n"
    "s_barrier\n"
    "s_nop 15\n"
    "global_store_dwordx4 %[lane16], v[208:211], s[52:53] sc1\n"
    "s_mov_b64 exec, s[76:77]\n"
    "global_store_dwordx4 %[lane16], v[212:215], s[52:53] offset:1024 sc1\n"
    "s_mov_b64 exec, -1\n"
    "global_store_dwordx4 %[lane16], v[200:203], s[50:51] sc1\n"
    "global_store_dwordx4 %[lane16], v[204:207], s[50:51] offset:1024 sc1\n"
    "s_add_u32 s50, s52, s67\n"
    "s_addc_u32 s51, s53, 0\n"
    "s_add_u32 s59, s56, 1\n"
    "s_and_b32 s59, s59, 3\n"
    "s_cselect_b32 s59, 0, 0x2000\n"
    "s_xor_b32 s52, s52, s59\n"
    "s_add_u32 s52, s52, 524288\n"
    "s_addc_u32 s53, s53, 0\n"
    "s_add_u32 s56, s56, 1\n"
    "s_mov_b64 s[40:41], s[42:43]\n"
    "s_add_u32 s59, s56, 1\n"
    "s_and_b32 s59, s59, 15\n"
    "s_cselect_b32 s59, 0, 0x4000\n"
    "s_add_u32 s42, s42, 0x8000\n"
    "s_xor_b32 s42, s42, s59\n"
    "s_barrier\n"
    "s_nop 15\n"
    "ds_read_b128 v[200:203], %[lane16] offset:12288\n"
    "ds_read_b128 v[204:207], %[lane16] offset:13312\n"
    "s_branch LloopA2_%=\n"
    "LlastA2_%=:\n"
    "global_store_dwordx4 %[lane16], v[208:211], s[52:53] sc1\n"
    "s_mov_b64 exec, s[76:77]\n"
    "global_store_dwordx4 %[lane16], v[212:215], s[52:53] offset:1024 sc1\n"
    "s_mov_b64 exec, -1\n"
    "global_store_dwordx4 %[lane16], v[200:203], s[50:51] sc1\n"
    "global_store_dwordx4 %[lane16], v[204:207], s[50:51] offset:1024 sc1\n"
    "s_add_u32 s50, s52, s67\n"
    "s_addc_u32 s51, s53, 0\n"
    "s_add_u32 s59, s56, 1\n"
    "s_and_b32 s59, s59, 3\n"
    "s_cselect_b32 s59, 0, 0x2000\n"
    "s_xor_b32 s52, s52, s59\n"
    "s_add_u32 s52, s52, 524288\n"
    "s_addc_u32 s53, s53, 0\n"
    "s_barrier\n"
    "ds_read_b128 v[200:203], %[lane16] offset:12288\n"
    "ds_read_b128 v[204:207], %[lane16] offset:13312\n"
    "s_waitcnt lgkmcnt(0)\n"
    "global_store_dwordx4 %[lane16], v[200:203], s[50:51] sc1\n"
    "global_store_dwordx4 %[lane16], v[204:207], s[50:51] offset:1024 sc1\n"
    "s_waitcnt vmcnt(0)\n"
    "s_barrier\n"
    "s_branch Lend_%=\n"
    "LmemretryA2_%=:\n"
    "s_cmp_eq_u32 s58, 0\n"
    "s_cbranch_scc1 LmemgoA2_%=\n"
    "s_sub_u32 s58, s58, 1\n"
    "s_cmp_eq_u32 %[rep], 0\n"
    "s_cbranch_scc1 LrsA2_%=\n"
    "s_sleep 4\n"
    "LrsA2_%=:\n"
    "global_load_dwordx4 v[152:155], v30, s[40:41] sc1\n"
    "s_nop 7\n"
    "v_mov_b32 v172, 0\n"
    "v_mov_b32 v173, 0\n"
    "v_mov_b32 v174, 0\n"
    "v_mov_b32 v175, 0\n"
    "v_mov_b32 v176, 0\n"
    "v_mov_b32 v177, 0\n"
    "v_mov_b32 v178, 0\n"
    "v_mov_b32 v179, 0\n"
    "s_waitcnt vmcnt(0)\n"
    "s_branch LmemA2_%=\n"
    "LA3_%=:\n"
    "v_mov_b32 v188, %[rb]\n"
    "v_mov_b32 v189, %[rd]\n"
    "s_lshl_b64 s[78:79], %[epoch], 8\n"
    "Lw4A3_%=:\n"
    "global_load_dwordx2 v[184:185], %[wfl], %[base] offset:-512 sc1\n"
    "s_waitcnt vmcnt(0)\n"
    "Lwc4A3_%=:\n"
    "v_xor_b32 v184, s78, v184\n"
    "v_xor_b32 v185, s79, v185\n"
    "v_subrev_u32 v184, 3, v184\n"
    "v_lshrrev_b32 v184, 3, v184\n"
    "v_or_b32 v184, v184, v185\n"
    "v_cmp_ne_u32 vcc, 0, v184\n"
    "s_cbranch_vccz Lwk4A3_%=\n"
    "s_cmp_eq_u32 s58, 0\n"
    "s_cbranch_scc1 Lwk4A3_%=\n"
    "s_sub_u32 s58, s58, 1\n"
    "s_sleep 6\n"
    "s_branch Lw4A3_%=\n"
    "Lwk4A3_%=:\n"
    "v_add_u32 v186, 4096, v188\n"
    "global_load_dwordx4 v[48:51], v186, %[base] offset:0 sc1\n"
    "global_load_dwordx4 v[52:55], v186, %[base] offset:1024 sc1\n"
    "global_load_dwordx4 v[56:59], v186, %[base] offset:2048 sc1\n"
    "v_add_u32 v186, 4096, v189\n"
    "global_load_dwordx4 v[104:107], v186, %[base] offset:0 sc1\n"
    "global_load_dwordx4 v[108:111], v186, %[base] offset:1024 sc1\n"
    "global_load_dwordx4 v[112:115], v186, %[base] offset:2048 sc1\n"
    "s_waitcnt vmcnt(2)\n"
    "global_load_dwordx2 v[184:185], %[wfl], %[base] offset:0 sc1\n"
    "v_add_u32 v186, 11264, v188\n"
    "global_load_dwordx4 v[76:79], v186, %[base] offset:0 sc1\n"
    "global_load_dwordx4 v[80:83], v186, %[base] offset:1024 sc1\n"
    "global_load_dwordx4 v[84:87], v186, %[base] offset:2048 sc1\n"
    "v_add_u32 v186, 11264, v189\n"
    "global_load_dwordx4 v[132:135], v186, %[base] offset:0 sc1\n"
    "global_load_dwordx4 v[136:139], v186, %[base] offset:1024 sc1\n"
    "global_load_dwordx4 v[140:143], v186, %[base] offset:2048 sc1\n"
    "s_waitcnt vmcnt(6)\n"
    "s_branch Lwc0A3_%=\n"
    "Lw0A3_%=:\n"
    "global_load_dwordx2 v[184:185], %[wfl], %[base] offset:0 sc1\n"
    "s_waitcnt vmcnt(0)\n"
    "Lwc0A3_%=:\n"
    "v_xor_b32 v184, s78, v184\n"
    "v_xor_b32 v185, s79, v185\n"
    "v_subrev_u32 v184, 4, v184\n"
    "v_lshrrev_b32 v184, 3, v184\n"
    "v_or_b32 v184, v184, v185\n"
    "v_cmp_ne_u32 vcc, 0, v184\n"
    "s_cbranch_vccz Lwk0A3_%=\n"
    "s_cmp_eq_u32 s58, 0\n"
    "s_cbranch_scc1 Lwk0A3_%=\n"
    "s_sub_u32 s58, s58, 1\n"
    "s_sleep 6\n"
    "s_branch Lw0A3_%=\n"
    "Lwk0A3_%=:\n"
    "v_add_u32 v186, 0, v188\n"
    "global_load_dwordx4 v[32:35], v186, %[base] offset:0 sc1\n"
    "global_load_dwordx4 v[36:39], v186, %[base] offset:1024 sc1\n"
    "global_load_dwordx4 v[40:43], v186, %[base] offset:2048 sc1\n"
    "global_load_dwordx4 v[44:47], v186, %[base] offset:3072 sc1\n"
    "v_add_u32 v186, 0, v189\n"
    "global_load_dwordx4 v[88:91], v186, %[base] offset:0 sc1\n"
    "global_load_dwordx4 v[92:95], v186, %[base] offset:1024 sc1\n"
    "global_load_dwordx4 v[96:99], v186, %[base] offset:2048 sc1\n"
    "global_load_dwordx4 v[100:103], v186, %[base] offset:3072 sc1\n"
    "v_add_u32 v186, 7168, v188\n"
    "global_load_dwordx4 v[60:63], v186, %[base] offset:0 sc1\n"
    "global_load_dwordx4 v[64:67], v186, %[base] offset:1024 sc1\n"
    "global_load_dwordx4 v[68:71], v186, %[base] offset:2048 sc1\n"
    "global_load_dwordx4 v[72:75], v186, %[base] offset:3072 sc1\n"
    "v_add_u32 v186, 7168, v189\n"
    "global_load_dwordx4 v[116:119], v186, %[base] offset:0 sc1\n"
    "global_load_dwordx4 v[120:123], v186, %[base] offset:1024 sc1\n"
    "global_load_dwordx4 v[124:127], v186, %[base] offset:2048 sc1\n"
    "global_load_dwordx4 v[128:131], v186, %[base] offset:3072 sc1\n"
    "s_setprio 3\n"
    "s_bfe_u32 s59, %[d0], 524288\n"
    "s_lshl_b32 s59, s59, 2\n"
    "s_getpc_b64 s[82:83]\n"
    "Lpca3_%=:\n"
    "s_add_u32 s82, s82, Lv1_%=-Lpca3_%=\n"
    "s_addc_u32 s83, s83, 0\n"
    "s_sub_u32 s82, s82, s59\n"
    "s_subb_u32 s83, s83, 0\n"
    "v_lshrrev_b32 v181, 1, %[lane16]\n"
    "v_lshrrev_b32 v182, 2, %[lane16]\n"
    "v_mov_b32 v186, %[go]\n"
    "LgoA3_%=:\n"
    "global_load_dwordx2 v[184:185], v186, %[base] sc1\n"
    "s_waitcnt vmcnt(0)\n"
    "v_readfirstlane_b32 s78, v184\n"
    "v_readfirstlane_b32 s79, v185\n"
    "s_nop 1\n"
    "s_lshr_b64 s[80:81], s[78:79], 8\n"
    "s_cmp_eq_u64 s[80:81], %[epoch]\n"
    "s_cbranch_scc1 LgokA3_%=\n"
    "s_cmp_eq_u32 s58, 0\n"
    "s_cbranch_scc1 LgokA3_%=\n"
    "s_sub_u32 s58, s58, 1\n"
    "s_sleep 2\n"
    "s_branch LgoA3_%=\n"
    "LgokA3_%=:\n"
    "s_bfe_u32 s79, s78, 0x20002\n"
    "s_and_b32 s78, s78, 3\n"
    "s_lshl_b32 s78, s78, 13\n"
    "s_lshl_b32 s79, s79, 13\n"
    "s_xor_b32 s42, s42, s78\n"
    "s_xor_b32 s44, s44, s78\n"
    "s_xor_b32 s46, s46, s79\n"
    "s_xor_b32 s48, s48, s79\n"
    "s_xor_b32 s38, s38, s79\n"
    "s_xor_b32 s67, s78, s79\n"
    "s_xor_b32 s67, s67, %[opf]\n"
    "LloopA3_%=:\n"
    "s_barrier\n"
    "global_load_dwordx4 v[156:159], v30, s[40:41] sc1\n"
    "s_waitcnt vmcnt(0)\n"
    "LmemA3_%=:\n"
    "ds_write_b128 %[voff16], v[156:159] offset:0\n"
    "v_smfmac_f32_16x16x64_bf16 v[172:175], v[156:159], v[56:63], %[idx]\n"
    "v_max3_u32 v184, v156, v157, v158\n"
    "v_max_u32 v184, v184, v159\n"
    "v_cmp_eq_u32 vcc, -1, v184\n"
    "v_smfmac_f32_16x16x64_bf16 v[176:179], v[156:159], v[112:119], %[idx]\n"
    "s_cbranch_vccnz LmemretryA3_%=\n"
    "LmemgoA3_%=:\n"
    "v_add_u32 v30, %[cpy], %[voff16]\n"
    "s_waitcnt lgkmcnt(0)\n"
    "s_barrier\n"
    "ds_read_b128 v[144:147], %[lane16] offset:0\n"
    "ds_read_b128 v[148:151], %[lane16] offset:1024\n"
    "ds_read_b128 v[152:155], %[lane16] offset:2048\n"
    "s_barrier\n"
    "ds_read_b128 v[160:163], %[lane16] offset:4096\n"
    "ds_read_b128 v[164:167], %[lane16] offset:5120\n"
    "ds_read_b128 v[168:171], %[lane16] offset:6144\n"
    "s_waitcnt lgkmcnt(5)\n"
    "v_smfmac_f32_16x16x64_bf16 v[172:175], v[144:147], v[32:39], %[idx]\n"
    "v_smfmac_f32_16x16x64_bf16 v[176:179], v[144:147], v[88:95], %[idx]\n"
    "s_waitcnt lgkmcnt(4)\n"
    "v_smfmac_f32_16x16x64_bf16 v[172:175], v[148:151], v[40:47], %[idx]\n"
    "v_smfmac_f32_16x16x64_bf16 v[176:179], v[148:151], v[96:103], %[idx]\n"
    "s_waitcnt lgkmcnt(3)\n"
    "v_smfmac_f32_16x16x64_bf16 v[172:175], v[152:155], v[48:55], %[idx]\n"
    "v_smfmac_f32_16x16x64_bf16 v[176:179], v[152:155], v[104:111], %[idx]\n"
    "s_waitcnt lgkmcnt(2)\n"
    "v_smfmac_f32_16x16x64_bf16 v[172:175], v[160:163], v[64:71], %[idx]\n"
    "s_waitcnt lgkmcnt(1)\n"
    "v_smfmac_f32_16x16x64_bf16 v[172:175], v[164:167], v[72:79], %[idx]\n"
    "s_waitcnt lgkmcnt(0)\n"
    "v_smfmac_f32_16x16x64_bf16 v[172:175], v[168:171], v[80:87], %[idx]\n"
    "v_smfmac_f32_16x16x64_bf16 v[176:179], v[160:163], v[120:127], %[idx]\n"
    "s_nop 2\n"
    "v_smfmac_f32_16x16x64_bf16 v[176:179], v[164:167], v[128:135], %[idx]\n"
    "s_nop 2\n"
    "v_smfmac_f32_16x16x64_bf16 v[176:179], v[168:171], v[136:143], %[idx]\n"
    "v_permlane32_swap_b32 v172, v174\n"
    "v_permlane32_swap_b32 v173, v175\n"
    "v_add_f32 v172, v172, v174\n"
    "v_add_f32 v173, v173, v175\n"
    "v_cvt_pk_bf16_f32 v184, v172, v173\n"
    "ds_write_b32 %[ra], v184 offset:7424\n"
    "s_cmp_eq_u32 s56, 63\n"
    "s_nop 0\n"
    "v_permlane32_swap_b32 v176, v178\n"
    "v_permlane32_swap_b32 v177, v179\n"
    "v_add_f32 v176, v176, v178\n"
    "v_add_f32 v177, v177, v179\n"
    "v_cvt_pk_bf16_f32 v185, v176, v177\n"
    "ds_write_b32 v182, v185 offset:12288\n"
    "v_mov_b32 v172, 0\n"
    "v_mov_b32 v173, 0\n"
    "v_mov_b32 v174, 0\n"
    "v_mov_b32 v175, 0\n"
    "v_mov_b32 v176, 0\n"
    "v_mov_b32 v177, 0\n"
    "v_mov_b32 v178, 0\n"
    "v_mov_b32 v179, 0\n"
    "s_waitcnt lgkmcnt(0)\n"
    "s_barrier\n"
    "s_barrier\n"
    "s_cbranch_scc1 LlastA3_%=\n"
    "s_xor_b32 s48, s42, s67\n"
    "s_barrier\n"
    "ds_read_b64 v[184:185], v181 offset:10240\n"
    "ds_read_b64 v[186:187], v181 offset:10752\n"
    "ds_read_b64 v[188:189], v181 offset:11264\n"
    "ds_read_b64 v[190:191], v181 offset:11776\n"
    "s_mov_b64 exec, s[60:61]\n"
    "s_waitcnt lgkmcnt(2)\n"
    "v_pk_add_f32 v[184:185], v[184:185], v[186:187]\n"
    "s_waitcnt lgkmcnt(0)\n"
    "v_pk_add_f32 v[188:189], v[188:189], v[190:191]\n"
    "s_nop 0\n"
    "v_pk_add_f32 v[184:185], v[184:185], v[188:189]\n"
    "s_nop 0\n"
    "v_cvt_pk_bf16_f32 v184, v184, v185\n"
    "v_min_u32 v184, -2, v184\n"
    "global_store_dword %[comb4], v184, s[42:43] sc1\n"
    "global_store_dword v183, v184, s[42:43] sc1\n"
    "global_store_dword %[comb4], v184, s[48:49] sc1\n"
    "s_mov_b64 exec, -1\n"
    "s_add_u32 s56, s56, 1\n"
    "s_mov_b64 s[40:41], s[42:43]\n"
    "s_add_u32 s59, s56, 1\n"
    "s_and_b32 s59, s59, 15\n"
    "s_cselect_b32 s59, 0, 0x4000\n"
    "s_add_u32 s42, s42, 0x8000\n"
    "s_xor_b32 s42, s42, s59\n"
    "s_setpc_b64 s[82:83]\n"
    ".rept 80\n"
    "s_nop 1\n"
    ".endr\n"
    "Lv1_%=:\n"
    "s_branch LloopA3_%=\n"
    "LlastA3_%=:\n"
    "v_mov_b32 v184, 64\n"
    "s_waitcnt vmcnt(0)\n"
    "s_barrier\n"
    "s_mov_b64 exec, 1\n"
    "global_store_dword %[rc], v184, %[base] sc1\n"
    "s_mov_b64 exec, -1\n"
    "s_waitcnt vmcnt(0)\n"
    "s_branch Lend_%=\n"
    "LmemretryA3_%=:\n"
    "s_cmp_eq_u32 s58, 0\n"
    "s_cbranch_scc1 LmemgoA3_%=\n"
    "s_sub_u32 s58, s58, 1\n"
    "s_cmp_eq_u32 %[rep], 0\n"
    "s_cbranch_scc1 LrsA3_%=\n"
    "s_sleep 4\n"
    "LrsA3_%=:\n"
    "global_load_dwordx4 v[156:159], v30, s[40:41] sc1\n"
    "s_nop 7\n"
    "v_mov_b32 v172, 0\n"
    "v_mov_b32 v173, 0\n"
    "v_mov_b32 v174, 0\n"
    "v_mov_b32 v175, 0\n"
    "v_mov_b32 v176, 0\n"
    "v_mov_b32 v177, 0\n"
    "v_mov_b32 v178, 0\n"
    "v_mov_b32 v179, 0\n"
    "s_waitcnt vmcnt(0)\n"
    "s_branch LmemA3_%=\n"
    "Lend_%=:\n"
    "Lcode_end:\n"
    "s_setprio 0\n"
    :
    : CHAIN_IN
    :
      "memory", "vcc", "scc", "v26", "v27", "v28", "v29", "v30", "v31", "v32", "v33", "v34", "v35", "v36", "v37", "v38", "v39", "v40", "v41", "v42",
      "v43", "v44", "v45", "v46", "v47", "v48", "v49", "v50", "v51", "v52", "v53", "v54", "v55", "v56", "v57", "v58", "v59", "v60", "v61", "v62",
      "v63", "v64", "v65", "v66", "v67", "v68", "v69", "v70", "v71", "v72", "v73", "v74", "v75", "v76", "v77", "v78", "v79", "v80", "v81", "v82",
      "v83", "v84", "v85", "v86", "v87", "v88", "v89", "v90", "v91", "v92", "v93", "v94", "v95", "v96", "v97", "v98", "v99", "v100", "v101", "v102",
      "v103", "v104", "v105", "v106", "v107", "v108", "v109", "v110", "v111", "v112", "v113", "v114", "v115", "v116", "v117", "v118", "v119", "v120", "v121", "v122",
      "v123", "v124", "v125", "v126", "v127", "v128", "v129", "v130", "v131", "v132", "v133", "v134", "v135", "v136", "v137", "v138", "v139", "v140", "v141", "v142",
      "v143", "v144", "v145", "v146", "v147", "v148", "v149", "v150", "v151", "v152", "v153", "v154", "v155", "v156", "v157", "v158", "v159", "v160", "v161", "v162",
      "v163", "v164", "v165", "v166", "v167", "v168", "v169", "v170", "v171", "v172", "v173", "v174", "v175", "v176", "v177", "v178", "v179", "v180", "v181", "v182",
      "v183", "v184", "v185", "v186", "v187", "v188", "v189", "v190", "v191", "v192", "v193", "v194", "v195", "v196", "v197", "v198", "v199", "v200", "v201", "v202",
      "v203", "v204", "v205", "v206", "v207", "v208", "v209", "v210", "v211", "v212", "v213", "v214", "v215", "v216", "v217", "v218", "v219", "v220", "v221", "v222",
      "v223", "v224", "v225", "v226", "v227", "v228", "v229", "v230", "v231", "v232", "v233", "v234", "v235", "v236", "v237", "v238", "v239", "v240", "v241", "v242",
      "v243", "v244", "v245", "v246", "v247", "v248", "v249", "v250", "v251", "s38", "s39", "s40", "s41", "s42", "s43", "s44", "s45", "s46", "s47", "s48",
      "s49", "s50", "s51", "s52", "s53", "s54", "s55", "s56", "s57", "s58", "s59", "s60", "s61", "s62", "s63", "s64", "s65", "s66", "s67", "s68",
      "s69", "s70", "s71", "s72", "s73", "s74", "s75", "s76", "s77", "s78", "s79", "s80", "s81", "s82", "s83");
}

__device__ __forceinline__ void dma16(const char* gsrc, u32 lds_dst) {
  u32 keep;
  asm volatile("s_mov_b32 %0, m0\n\ts_mov_b32 m0, %2\n\ts_nop 0\n\tglobal_load_lds_dwordx4 %1, off\n\ts_mov_b32 m0, %0" : "=&s"(keep) : "v"(gsrc), "s"(lds_dst));
}

template <bool ISV, int S0, int NS, bool KEEP, class F, class G>
__device__ __forceinline__ v2u form_weights(char* ws, rsrc_t rs, LAS char* lds, const u16* Wn, const u16* Wd, int gi, u32 myflag, u32 wo,
                                            v4u (&wf)[14], v4u (&wp)[14], F last, G fetch, int w, int lane) {
  constexpr int EX = 131072;
  const int G0 = (gi % 56) * 4, kh = w >> 2, h = (G0 + (w & 3)) / 14, hi = lane >> 5, r32 = lane & 31;
  const int h0 = G0 / 14, two = (G0 + 3) / 14 - h0, rmap = row_map(r32);
#pragma unroll
  for (int i = 0; i < 8; ++i) {
    const int p = (w * 8 + i) * 64 + lane;
    if (ISV) {
      const int e = p >> 4, ch = (p & 15) ^ ((e & 3) << 2), Gs = G0 + (ch >> 2);
      dma16((const char*)(Wn + (size_t)((Gs / 14) * E + e) * D + (Gs % 14) * 32 + (ch & 3) * 8), (NS & 1 ? 0 : 65536) + (w * 8 + i) * 1024);
    } else {
      const int row = p >> 5, Gs = G0 + (row >> 5);
      dma16((const char*)(Wn + ((size_t)((Gs % 14) * 32 + (row & 31)) * H + Gs / 14) * E + ((p & 31) ^ (row & 15)) * 8), (NS & 1 ? 0 : 65536) + (w * 8 + i) * 1024);
    }
  }
  const int drow = (w & 3) * 8 + hi;
  const char* dsrc = (const char*)(Wd + ((size_t)(kh * 224 + S0 * 32 + drow) * H + h0) * E);
  auto piece = [&](int st, int i) {
    const u32 dst = ((NS - st) & 1) * 65536 + kh * 16384 + (w & 3) * 4096 + i * 1024;
    const char* p = dsrc + (size_t)(st * 32 + 2 * i) * (H * E * 2) + ((r32 ^ ((drow + 2 * i) & 15)) * 16);
    dma16(p, dst);
    if (two) dma16(p + E * 2, dst + 32768);
  };
#pragma unroll
  for (int i = 0; i < 4; ++i) piece(0, i);
  {
    const int t = min((gi % 28) * 8 + w, (NS - 1) * 64 - 1), row = (t & 1) * 224 + (S0 + 1) * 32 + (t >> 1);
    u32 keep;
    asm volatile("s_mov_b32 %0, m0\n\ts_mov_b32 m0, %2\n\ts_nop 0\n\tglobal_load_lds_dword %1, off\n\ts_mov_b32 m0, %0"
                 : "=&s"(keep) : "v"((const char*)Wd + (size_t)row * 8192 + (h0 >> 3) * 4096 + lane * 64), "s"(EX + 16384 + w * 256));
  }
  const v2u ep = read_epoch(ws);
  asm volatile("s_waitcnt vmcnt(1)" ::: "memory");
  bar();
  if (w == 0 && lane == 0) st8(rs, myflag, ep);
  v4u nf[16];
  LAS char* ns = lds + (NS & 1 ? 0 : 65536);
#pragma unroll
  for (int k = 0; k < 16; ++k) {
    if (ISV) {
      const int q = (lane & 15) >> 2;
      LAS char* p = ns + (16 * k + 8 * hi + q) * 256 + (((w & 3) ^ q) * 64) + ((lane >> 4) & 1) * 32 + (lane & 3) * 8;
      const v2u lo = __builtin_bit_cast(v2u, __builtin_amdgcn_ds_read_tr16_b64_v4i16((LAS s4*)p));
      const v2u up = __builtin_bit_cast(v2u, __builtin_amdgcn_ds_read_tr16_b64_v4i16((LAS s4*)(p + 1024)));
      nf[k] = (v4u){lo.x, lo.y, up.x, up.y};
    } else {
      nf[k] = *(LAS v4u*)(ns + ((w & 3) * 32 + rmap) * 512 + (((2 * k + hi) ^ (rmap & 15)) * 16));
    }
  }
  bar();
  LAS char* blk[8];
#pragma unroll
  for (int k = 0; k < 8; ++k) blk[k] = lds + ((h - h0) * 2 + kh) * 16384 + rmap * 512 + (((2 * k + hi) ^ (rmap & 15)) * 16);
  f16v acc[2];
  auto emit = [&](int st, int u) {
    f16v& a = acc[st & 1];
    const float sc = ISV ? 1.f : 0.0625f * 1.4426950408889634f;
    const v4u t = {pk(a[8 * u] * sc, a[8 * u + 1] * sc), pk(a[8 * u + 2] * sc, a[8 * u + 3] * sc), pk(a[8 * u + 4] * sc, a[8 * u + 5] * sc), pk(a[8 * u + 6] * sc, a[8 * u + 7] * sc)};
    if (KEEP) { wf[2 * st + u] = t; *(LAS v4u*)(lds + EX + (st & 1) * 16384 + w * 2048 + u * 1024 + lane * 16) = t; }
    const auto p = __builtin_amdgcn_permlane32_swap(t.x, t.z, false, false), q = __builtin_amdgcn_permlane32_swap(t.y, t.w, false, false);
    st16(rs, wo + (2 * (S0 + st) + u) * 512, (v4u){p[0], p[1], q[0], q[1]});
  };
  auto take = [&](int st, int u) { wp[2 * st + u] = *(LAS v4u*)(lds + EX + (st & 1) * 16384 + (w ^ 4) * 2048 + u * 1024 + lane * 16); };
#pragma unroll
  for (int st = 0; st < NS; ++st) {
    auto rd = [&](int k) { return *(LAS v4u*)(blk[k & 7] + ((NS - st) & 1) * 65536 + (k >> 3) * 256); };
    acc[st & 1] = (f16v){};
    v4u df[16];
#pragma unroll
    for (int k = 0; k < 4; ++k) df[k] = rd(k);
    __builtin_amdgcn_sched_barrier(0);
#pragma unroll
    for (int k = 0; k < 16; ++k) {
      acc[st & 1] = mfma32(df[k], nf[k], acc[st & 1]);
      if (k < 12) df[k + 4] = rd(k + 4);
      if (st + 1 < NS) { if (!(k & 1) && k < 8) piece(st + 1, k >> 1); }
      else last(k);
      if (st > 0 && (k == 9 || k == 12)) emit(st - 1, k == 12);
      if (KEEP && st > 1 && k >= 14) take(st - 2, k - 14);
      __builtin_amdgcn_sched_barrier(0);
    }
    if (st + 1 < NS) {
      if (st == 0) asm volatile("s_waitcnt vmcnt(0)" ::: "memory"); else asm volatile("s_waitcnt vmcnt(2)" ::: "memory");
      bar();
    }
  }
  if (KEEP) bar();
  emit(NS - 1, 0); emit(NS - 1, 1);
  if (KEEP) { take(NS - 2, 0); take(NS - 2, 1); fetch(ep); }
  else asm volatile("s_waitcnt vmcnt(0)" ::: "memory");
  bar();
  if (w == 0 && lane == 0) st8(rs, myflag, (v2u){ep.x | NS, ep.y});
  if (KEEP) { take(NS - 1, 0); take(NS - 1, 1); }
  return ep;
}

template <bool ISV>
__device__ __forceinline__ u32 frag_off(int gi, int w, int lane) {
  const int G = (gi % 56) * 4 + (w & 3), c = ISV ? lane & 31 : row_map(lane & 31);
  return O_WF + ((ISV ? 448 : 0) + G * 2 + (c >> 4)) * 14336 + (w >> 2) * 7168 + ((lane >> 5) * 16 + (c & 15)) * 16;
}

template <bool ISV>
__device__ __forceinline__ v2u help_role(char* ws, rsrc_t rs, LAS char* lds, const u16* Wn, const u16* Wd, int gi, int w, int lane) {
  v4u none[14];
  return form_weights<ISV, 4, 3, false>(ws, rs, lds, Wn, Wd, gi, O_PREP + (gemm_wg(gi) - 4) * 128, frag_off<ISV>(gi, w, lane), none, none, [](int) {}, [](v2u) {}, w, lane);
}

template <bool ISV>
__device__ __forceinline__ v2u gemm_role(char* ws, rsrc_t rs, LAS char* lds, const u16* x, const u16* Wn, const u16* Wd, int gi, int w, int lane) {
  const int G = (gi % 56) * 4 + (w & 3), kh = w >> 2, h = G / 14, gb = G % 14, hi = lane >> 5, r32 = lane & 31;
  const char* xsrc[7];
#pragma unroll
  for (int i = 0; i < 7; ++i) {
    const int p = (w * 7 + i) * 64 + lane, row = p / 56;
    xsrc[i] = (const char*)x + (p ^ ((row >> 1) & 7)) * 16;
  }
  const u32 myflag = O_PREP + gemm_wg(gi) * 128, wo = frag_off<ISV>(gi, w, lane);
  v4u wf[14], wp[14];
  u32 cls = 0;
  const v2u ep = form_weights<ISV, 0, 4, true>(ws, rs, lds, Wn, Wd, gi, myflag, wo, wf, wp, [&](int k) { if (k < 7) dma16(xsrc[k], w * 7168 + k * 1024); },
                                [&](v2u ep) {
                                  wait_epoch(rs, myflag - 512, ep, 3);
#pragma unroll
                                  for (int k = 8; k < 14; ++k) { wf[k] = ld16(rs, wo + k * 512); wp[k] = ld16(rs, wo + (kh ? -7168 : 7168) + k * 512); }
                                }, w, lane);
#pragma unroll
  for (int k = 8; k < 14; ++k) {
    const auto p = __builtin_amdgcn_permlane32_swap(wf[k].x, wf[k].y, false, false), q = __builtin_amdgcn_permlane32_swap(wf[k].z, wf[k].w, false, false);
    wf[k] = (v4u){p[0], q[0], p[1], q[1]};
    const auto r = __builtin_amdgcn_permlane32_swap(wp[k].x, wp[k].y, false, false), t = __builtin_amdgcn_permlane32_swap(wp[k].z, wp[k].w, false, false);
    wp[k] = (v4u){r[0], t[0], r[1], t[1]};
  }
  u32 xo[2][4];
#pragma unroll
  for (int p = 0; p < 2; ++p)
#pragma unroll
    for (int k = 0; k < 4; ++k) {
      const int cx = 28 * (kh ^ p) + 2 * k + hi;
      xo[p][k] = kh * 28672 + r32 * 896 + ((cx & ~7) + ((cx & 7) ^ ((r32 >> 1) & 7))) * 16;
    }
  u32 o0;
  if (ISV) o0 = O_VX + ((gb * 4 + (r32 >> 3)) * 16 + (h >> 1)) * 1024 + (hi * 16 + (h & 1) * 8 + (r32 & 7)) * 16;
  else o0 = O_KX + (h * 4 + ((r32 >> 2) & 3)) * 16384 + gb * 512 + hi * 128 + ((r32 & 3) + 4 * (r32 >> 4)) * 16;
#pragma unroll 1
  for (int sp = 0; sp < 32; ++sp) {
    LAS char* xb = lds + (sp & 1) * 57344;
    const int nx = min(sp + 1, 31);
    f16v am = {};
    v4u xf[28];
    auto rd = [&](int i) { return *(LAS v4u*)(xb + xo[i >= 14][(i % 14) & 3] + ((i % 14) >> 2) * 128); };
#pragma unroll
    for (int i = 0; i < 4; ++i) xf[i] = rd(i);
    __builtin_amdgcn_sched_barrier(0);
#pragma unroll
    for (int i = 0; i < 28; ++i) {
      const v4u wv = i < 14 ? wf[i % 14] : wp[i % 14];
      am = ISV ? mfma32(xf[i], wv, am) : mfma32(wv, xf[i], am);
      if (i + 4 < 28) xf[i + 4] = rd(i + 4);
      if (i % 3 == 1 && i < 21) dma16(xsrc[i / 3] + (size_t)nx * 57344, ((sp + 1) & 1) * 57344 + w * 7168 + (i / 3) * 1024);
      if (i == 14) {
        asm volatile("s_waitcnt vmcnt(5)\n\ts_barrier" ::: "memory");
        if (sp > 1 && w == 0 && lane == 0) st4(rs, O_GFLAG + gi * 128, 2 * sp);
      }
      __builtin_amdgcn_sched_barrier(0);
    }
    if (sp == 0) cls = wait_epoch(rs, O_GO + (gi & 63) * 128, ep);
    const u32 o = o0 + seg_off(2 * sp + kh, cls);
    if (ISV) {
      st16(rs, o, (v4u){pk(am[0], am[1]), pk(am[2], am[3]), pk(am[8], am[9]), pk(am[10], am[11])});
      st16(rs, o + 512, (v4u){pk(am[4], am[5]), pk(am[6], am[7]), pk(am[12], am[13]), pk(am[14], am[15])});
    } else {
      st16(rs, o, (v4u){pk(am[0], am[1]), pk(am[2], am[3]), pk(am[4], am[5]), pk(am[6], am[7])});
      st16(rs, o + 256, (v4u){pk(am[8], am[9]), pk(am[10], am[11]), pk(am[12], am[13]), pk(am[14], am[15])});
    }
    if (sp == 0) asm volatile("s_waitcnt vmcnt(0)" ::: "memory"); else asm volatile("s_waitcnt vmcnt(2)" ::: "memory");
    bar();
    if (sp == 0 && w == 0 && lane == 0) st4(rs, O_GFLAG + gi * 128, 2);
  }
  asm volatile("s_waitcnt vmcnt(0)" ::: "memory");
  bar();
  if (w == 0 && lane == 0) st4(rs, O_GFLAG + gi * 128, 64);
  return ep;
}

__device__ __forceinline__ void ffn_role(rsrc_t rs, LAS char* lds, const u16* x, const u16* wg, const u16* wu, const u16* wd, int fi, int sel, int w, int lane, int tid) {
  LAS float* red = (LAS float*)lds;
  LAS float* hs = (LAS float*)(lds + 16384);
  const int dg = tid >> 3, fc = (tid & 7) * 8;
  v4u a[7], c[7], dn[8];
  float xv[7];
#pragma unroll
  for (int i = 0; i < 7; ++i) {
    const int d = i * 64 + dg;
    xv[i] = bflo(x[(size_t)sel * D + d]);
    a[i] = *(const v4u*)(wg + (size_t)d * F + fi * 64 + fc); c[i] = *(const v4u*)(wu + (size_t)d * F + fi * 64 + fc);
  }
#pragma unroll
  for (int i = 0; i < 8; ++i) dn[i] = *(const v4u*)(wd + (size_t)(fi * 64 + i * 8 + w) * D + min(lane, 55) * 8);
  float ag[8] = {}, au[8] = {};
#pragma unroll
  for (int i = 0; i < 7; ++i)
#pragma unroll
    for (int k = 0; k < 4; ++k) {
      ag[2 * k] += xv[i] * bflo(a[i][k]); ag[2 * k + 1] += xv[i] * bfhi(a[i][k]);
      au[2 * k] += xv[i] * bflo(c[i][k]); au[2 * k + 1] += xv[i] * bfhi(c[i][k]);
    }
#pragma unroll
  for (int k = 0; k < 8; ++k) {
    ag[k] = xsum(ag[k] + __shfl_xor(ag[k], 8)); au[k] = xsum(au[k] + __shfl_xor(au[k], 8));
    if (lane < 8) { red[w * 64 + fc + k] = ag[k]; red[512 + w * 64 + fc + k] = au[k]; }
  }
  bar();
  if (tid < 64) {
    float gs = 0.f, us = 0.f;
#pragma unroll
    for (int i = 0; i < 8; ++i) { gs += red[i * 64 + tid]; us += red[512 + i * 64 + tid]; }
    gs = rnd(gs); us = rnd(us);
    hs[tid] = rnd(rnd(gs * sigmoidf(gs)) * us);
  }
  bar();
  float ay[8] = {};
#pragma unroll
  for (int i = 0; i < 8; ++i) {
    const float hv = hs[i * 8 + w];
#pragma unroll
    for (int k = 0; k < 4; ++k) { ay[2 * k] += hv * bflo(dn[i][k]); ay[2 * k + 1] += hv * bfhi(dn[i][k]); }
  }
  if (lane < 56) {
#pragma unroll
    for (int k = 0; k < 8; ++k) red[w * 448 + lane * 8 + k] = ay[k];
  }
  bar();
  if (tid < 448) {
    float s = 0.f;
#pragma unroll
    for (int i = 0; i < 8; ++i) s += red[i * 448 + tid];
    st4(rs, O_YP + (fi * 448 + tid) * 4, __float_as_uint(s));
  }
  asm volatile("s_waitcnt vmcnt(0)" ::: "memory");
  bar();
  if (tid == 0) st4(rs, O_FFLAG + fi * 128, 1);
}

__device__ __forceinline__ void stage_x(LAS char* lds, const u16* x, int s, int tid) {
  bar();
  for (int c = tid; c < 1792; c += 512) {
    const int l = c & 63, f = c >> 6, qt = f / 14, ks = f % 14;
    *(LAS v4u*)(lds + c * 16) = *(const v4u*)(x + (size_t)(s * 32 + qt * 16 + (l & 15)) * D + ks * 32 + (l >> 4) * 8);
  }
}

__device__ __forceinline__ void score_unit(rsrc_t rs, LAS char* lds, const u16* x, int s, int fb, int w, int lane, int tid) {
  const int g = lane >> 4, n = lane & 15;
  stage_x(lds, x, s, tid);
  wait_flags(rs, O_GFLAG, 112, s + 1, w, lane);
  const u32 o = O_KX + seg_off(s, fb) + (8 * w + (n >> 3)) * 16384 + g * 128 + (n & 7) * 16;
  v4u kf[2][14];
#pragma unroll
  for (int ks = 0; ks < 14; ++ks) kf[0][ks] = ld16(rs, o + ks * 512);
#pragma unroll
  for (int u = 0; u < 4; ++u) {
    if (u < 3) {
#pragma unroll
      for (int ks = 0; ks < 14; ++ks) kf[(u + 1) & 1][ks] = ld16(rs, o + (u + 1) * 32768 + ks * 512);
    }
    __builtin_amdgcn_sched_barrier(0);
    f4 s0 = {}, s1 = {};
#pragma unroll
    for (int ks = 0; ks < 14; ++ks) {
      s0 = mfma16(kf[u & 1][ks], *(LAS v4u*)(lds + ks * 1024 + lane * 16), s0);
      s1 = mfma16(kf[u & 1][ks], *(LAS v4u*)(lds + (14 + ks) * 1024 + lane * 16), s1);
    }
    const u32 od = O_SX + (s * 16 + 2 * w + (u >> 1)) * 3072 + ((2 * (u & 1) + (g >> 1)) * 16 + n) * 16;
    if (!(g & 1)) st16(rs, od, __builtin_bit_cast(v4u, s0));
    st16(rs, od + 1024 + (g & 1) * 1024, __builtin_bit_cast(v4u, s1));
    __builtin_amdgcn_sched_barrier(0);
  }
  asm volatile("s_waitcnt vmcnt(0)" ::: "memory");
  bar();
  if (tid == 0) st4(rs, O_SFLAG + s * 128, 1);
}

template <int Q0, int NQ>
__device__ __forceinline__ void attn_unit(char* ws, rsrc_t rs, LAS char* lds, const u16* x, u16* out, int s, int cq, int fb, int sel, float rsel,
                                          int w, int lane, int tid) {
  constexpr int XL = 0, PX = 28672, PM = PX + 32768;
  const int g = lane >> 4, n = lane & 15;
  stage_x(lds, x, s, tid);
  wait_flags(rs, O_SFLAG + s * 128, 1, 1, w, lane);
  f4 ex[2][3];
#pragma unroll
  for (int hh = 0; hh < 2; ++hh)
#pragma unroll
    for (int t = Q0; t < (Q0 + NQ == 2 ? 3 : 1); ++t) ex[hh][t] = __builtin_bit_cast(f4, ld16(rs, O_SX + ((s * 16 + 2 * w + hh) * 3 + t) * 1024 + lane * 16));
  const int ct = cq * 7 + min(w, 6), cg = 2 * ct + (n >> 3);
  v4u vx[16];
#pragma unroll
  for (int h = 0; h < 16; ++h) vx[h] = ld16(rs, O_VX + seg_off(s, fb) + (cg * 16 + (h >> 1)) * 1024 + (g * 16 + (h & 1) * 8 + (n & 7)) * 16);
  f4 yv = {};
  if ((sel >> 5) == s) {
    wait_flags(rs, O_FFLAG, 28, 1, w, lane);
    const float sg = rnd(sigmoidf(rsel));
#pragma unroll 7
    for (int i = 0; i < 28; ++i) yv += __builtin_bit_cast(f4, ld16(rs, O_YP + (i * 448 + ct * 16 + g * 4) * 4));
#pragma unroll
    for (int r = 0; r < 4; ++r) yv[r] = rnd(rnd(yv[r]) * sg);
  }
  float gt[2], mx[2][2], lx[2][2];
  f4 ob[2];
#pragma unroll
  for (int qt = Q0; qt < Q0 + NQ; ++qt) {
    const int c = ct * 16 + g * 4;
    gt[qt] = __uint_as_float(ld4(rs, O_GATE + (s * 32 + qt * 16 + n) * 4));
    const v2u xv = *(LAS v2u*)(lds + XL + (qt * 14 + (c >> 5)) * 1024 + (((c & 31) >> 3) * 16 + n) * 16 + (c & 7) * 2);
    const f4 bx = {bflo(xv.x), bfhi(xv.x), bflo(xv.y), bfhi(xv.y)};
    const float og = rnd(1.f - gt[qt]);
#pragma unroll
    for (int r = 0; r < 4; ++r) ob[qt][r] = rnd(og * (s * 32 + qt * 16 + n == sel ? rnd(bx[r] + yv[r]) : bx[r]));
#pragma unroll
    for (int hh = 0; hh < 2; ++hh) {
      f4& dg = ex[hh][2 * qt];
#pragma unroll
      for (int r = 0; r < 4; ++r) if (4 * g + r > n) dg[r] = -INFINITY;
      float m = fmaxf(fmaxf(dg[0], dg[1]), fmaxf(dg[2], dg[3]));
      if (qt) m = fmaxf(m, fmaxf(fmaxf(ex[hh][1][0], ex[hh][1][1]), fmaxf(ex[hh][1][2], ex[hh][1][3])));
      m = fmaxf(xmax(m), -3.0e38f);
      float l = 0.f;
#pragma unroll
      for (int r = 0; r < 4; ++r) {
        dg[r] = __builtin_amdgcn_exp2f(dg[r] - m); l += dg[r];
        if (qt) { ex[hh][1][r] = __builtin_amdgcn_exp2f(ex[hh][1][r] - m); l += ex[hh][1][r]; }
      }
      mx[qt][hh] = m; lx[qt][hh] = l;
    }
  }
  asm volatile("s_waitcnt vmcnt(0)" ::: "memory");
  if (s >= 61) wait_flags(rs, O_CFLAG, 64, 64, w, lane); else wait_flags(rs, O_PFLAG + ((s * 4 + cq) & 15) * 128, 1, s + 1, w, lane);
  f4 sm[2] = {};
  v4u wv[4];
  {
    const int key = 4 * ((n >> 2) & 1) + 2 * (n >> 3) + (n & 1);
    const u32 o = O_KM + by_off(s, fb) + (2 * w + ((n >> 1) & 1)) * 16384 + g * 128 + key * 16;
    v4u km[14];
#pragma unroll
    for (int ks = 0; ks < 14; ++ks) km[ks] = ld16(rs, o + ks * 512);
#pragma unroll
    for (int ks = 0; ks < 14; ++ks)
#pragma unroll
      for (int qt = Q0; qt < Q0 + NQ; ++qt) sm[qt] = mfma16(km[ks], *(LAS v4u*)(lds + XL + (qt * 14 + ks) * 1024 + lane * 16), sm[qt]);
    __builtin_amdgcn_sched_barrier(0);
    const u32 ow = O_W + by_off(s, fb) + (cg >> 2) * 16384 + (cg & 3) * 2048 + (g * 16 + (n & 7)) * 4;
#pragma unroll
    for (int u = 0; u < 4; ++u) wv[u] = (v4u){ld4(rs, ow + u * 512), ld4(rs, ow + u * 512 + 32), ld4(rs, ow + u * 512 + 256), ld4(rs, ow + u * 512 + 288)};
  }
#pragma unroll
  for (int qt = Q0; qt < Q0 + NQ; ++qt) {
    u32 pm[2];
#pragma unroll
    for (int hh = 0; hh < 2; ++hh) {
      float m0 = sm[qt][2 * hh], m1 = sm[qt][2 * hh + 1];
      const float m = xmax(fmaxf(fmaxf(m0, m1), mx[qt][hh]));
      m0 = __builtin_amdgcn_exp2f(m0 - m); m1 = __builtin_amdgcn_exp2f(m1 - m);
      const float al = __builtin_amdgcn_exp2f(mx[qt][hh] - m);
      const float il = __builtin_amdgcn_rcpf(xsum(m0 + m1 + al * lx[qt][hh])), sc = al * il;
      const f4 a = ex[hh][qt] * sc, c = qt ? ex[hh][2] * sc : (f4){};
      *(LAS v4u*)(lds + PX + ((2 * w + hh) * 2 + qt) * 1024 + lane * 16) = (v4u){pk(a[0], a[1]), pk(a[2], a[3]), pk(c[0], c[1]), pk(c[2], c[3])};
      pm[hh] = pk(m0 * il, m1 * il);
    }
    *(LAS v2u*)(lds + PM + ((w >> 1) * 2 + qt) * 1024 + lane * 16 + (w & 1) * 8) = (v2u){pm[0], pm[1]};
  }
  bar();
  if (w < 7) {
    f4 acc[2] = {};
#pragma unroll
    for (int h = 0; h < 16; ++h)
#pragma unroll
      for (int qt = Q0; qt < Q0 + NQ; ++qt) acc[qt] = mfma16(vx[h], *(LAS v4u*)(lds + PX + (h * 2 + qt) * 1024 + lane * 16), acc[qt]);
#pragma unroll
    for (int u = 0; u < 4; ++u)
#pragma unroll
      for (int qt = Q0; qt < Q0 + NQ; ++qt) acc[qt] = mfma16(wv[u], *(LAS v4u*)(lds + PM + (u * 2 + qt) * 1024 + lane * 16), acc[qt]);
#pragma unroll
    for (int qt = Q0; qt < Q0 + NQ; ++qt) {
      float o[4];
#pragma unroll
      for (int r = 0; r < 4; ++r) o[r] = rnd(gt[qt] * rnd(acc[qt][r])) + ob[qt][r];
      *(v2u*)(out + (size_t)(s * 32 + qt * 16 + n) * D + ct * 16 + g * 4) = (v2u){pk(o[0], o[1]), pk(o[2], o[3])};
    }
  }
}
}

__global__ void __launch_bounds__(512, 2) k_main(const u16* __restrict__ x, const u16* __restrict__ mem0, const u16* __restrict__ Wq,
                                                 const u16* __restrict__ Wk, const u16* __restrict__ Wv, const u16* __restrict__ Wo,
                                                 const u16* __restrict__ wr, const u16* __restrict__ wg, const u16* __restrict__ wu,
                                                 const u16* __restrict__ wd, const u16* __restrict__ gw, u16* __restrict__ out, char* ws,
                                                 u32 d0, u32 d1) {
  extern __shared__ __attribute__((aligned(16))) char lds_[];
  LAS char* lds = (LAS char*)lds_;
  const int b = blockIdx.x, tid = threadIdx.x, lane = tid & 63, w = __builtin_amdgcn_readfirstlane(tid >> 6);
  const int xc = b & 7, j = b >> 3;
  const rsrc_t rs = __builtin_amdgcn_make_buffer_rsrc(ws, 0, WS_USED, 0x00020000);
  const char* code; u32 code_len;
  asm volatile("s_getpc_b64 %0\nLcode_begin:\n\ts_mov_b32 %1, Lcode_end-Lcode_begin" : "=s"(code), "=s"(code_len));
  v2u ep;
  if (xc >= 4 && j < 28) {
    const int gi = (xc - 4) * 28 + j;
    ep = gi < 56 ? gemm_role<false>(ws, rs, lds, x, Wq, Wk, gi, w, lane) : gemm_role<true>(ws, rs, lds, x, Wo, Wv, gi, w, lane);
    if (j >= 16 || (j >= 8 && xc == 7)) return;
  } else if (j < 28) {
    const int gi = xc * 28 + j;
    ep = gi < 56 ? help_role<false>(ws, rs, lds, Wq, Wk, gi, w, lane) : help_role<true>(ws, rs, lds, Wo, Wv, gi, w, lane);
  } else {
    const u32 cw = *(const u32*)(code + min((u32)(w * 8192 + lane * 128), code_len));
    ep = read_epoch(ws);
    if (xc == 0 && w == 0) probe_unit(ws, rs, j - 28, lane);
    if (b == 225 && tid < 248) st4(rs, O_GFLAG + tid * 128, 0);
    if (b == 227 && tid < 92) st4(rs, O_SFLAG + tid * 128, 0);
    if (b == 226 && tid < 448) {
      const v4u m0 = *(const v4u*)(mem0 + (tid & 7) * D + (tid >> 3) * 8);
      st16(rs, O_MEM0 + tid * 16, (v4u){min(m0.x, ~1u), min(m0.y, ~1u), min(m0.z, ~1u), min(m0.w, ~1u)});
    }
    asm volatile("s_waitcnt vmcnt(0)" :: "v"(cw) : "memory");
    bar();
    if (tid == 0) st8(rs, O_PREP + b * 128, ep);
    if (xc >= 4) {
#pragma unroll 1
      for (int u = ((j - 28) * 4 + xc - 4) * 8 + w; u < 256; u += 128) setup_unit(x, wr, gw, rs, u, lane);
      asm volatile("s_waitcnt vmcnt(0)" ::: "memory");
      bar();
      if (tid == 0) st8(rs, O_PREP + b * 128, (v2u){ep.x | 1, ep.y});
    }
  }
  if (xc == 0 || xc == 2) { chain_role(ws, lds, 2 * j + (xc >> 1), w, lane, tid, d0, d1, rs, ep); return; }
  if (xc == 1 && j == 31) { collect(rs, ep, w, lane); return; }
  const u32 cls = wait_epoch(rs, O_GO + (b & 63) * 128, ep);
  if (xc == 1 && j < 16) { wait_flags(rs, O_ZFLAG, 47, 1, w, lane); score_role(ws, j, w, lane, d1, cls); return; }
  if (xc < 4) poison_role(rs, xc == 1 ? 16 + j : j, cls, tid);
  if (xc >= 4 && j >= 28) {
    for (int s = (j - 28) * 4 + (xc - 4); s < 64; s += 16) score_unit(rs, lds, x, s, cls, w, lane, tid);
  } else if (xc != 1 || j < 24) {
    int sel; float rsel;
    wait_epoch(rs, O_PREP + ((28 + ((lane >> 2) & 3)) * 8 + 4 + (lane & 3)) * 128, ep, 1);
    route(rs, lane, sel, rsel);
    if (xc == 3) {
      if (j >= 4) ffn_role(rs, lds, x, wg, wu, wd, j - 4, sel, w, lane, tid);
      for (int s = j >> 2; s < 56; s += 8) attn_unit<0, 2>(ws, rs, lds, x, out, s, j & 3, cls, sel, rsel, w, lane, tid);
    } else {
      const int s = xc == 1 ? 63 : 56 + (j >> 3) * 4 + (xc - 4);
      if (j & 4) attn_unit<1, 1>(ws, rs, lds, x, out, s, j & 3, cls, sel, rsel, w, lane, tid);
      else attn_unit<0, 1>(ws, rs, lds, x, out, s, j & 3, cls, sel, rsel, w, lane, tid);
    }
  }
}

#define PK4(a, b, c, d) ((u32)(a) / 8 | (u32)(b) / 8 << 8 | (u32)(c) / 8 << 16 | (u32)(d) / 8 << 24)
#define DELAY0 PK4(120, 160, 40, 96)
#define DELAY1 PK4(296, 0, 200, 0)

extern "C" void kernel_launch(void* const* d_in, const int* in_sizes, int n_in, void* d_out, int out_size, void* d_ws, size_t ws_size,
                              hipStream_t stream) {
  char* ws = (char*)(((size_t)d_ws + 2 * MB - 1) & ~(size_t)(2 * MB - 1));
  (void)hipFuncSetAttribute((const void*)k_main, hipFuncAttributeMaxDynamicSharedMemorySize, LDS_BYTES);
  k_main<<<256, 512, LDS_BYTES, stream>>>((const u16*)d_in[0], (const u16*)d_in[1], (const u16*)d_in[2], (const u16*)d_in[3], (const u16*)d_in[4],
                                          (const u16*)d_in[5], (const u16*)d_in[8], (const u16*)d_in[9], (const u16*)d_in[10],
                                          (const u16*)d_in[11], (const u16*)d_in[12], (u16*)d_out, ws, DELAY0, DELAY1);
}
```
